```python
import torch

def _softmax(x, axis=-1):
    return torch.softmax(x, dim=axis)

def _gelu(x):
    return torch.nn.functional.gelu(x, approximate='tanh')

def _silu(x):
    return torch.nn.functional.silu(x)

def _sigmoid(x):
    return torch.sigmoid(x)

def _layernorm(x, axis=-1, eps=1e-05):
    m = x.mean(dim=axis, keepdim=True)
    v = x.var(dim=axis, keepdim=True, unbiased=False)
    return (x - m) / torch.sqrt(v + eps)

def _scatter_add(tgt, idx, src, axis=0):
    return tgt.clone().index_add_(axis, idx, src)

def _segment_sum(src, seg, n):
    out = torch.zeros((n,) + src.shape[1:], dtype=src.dtype)
    return out.index_add_(0, seg, src)

def _pad(x, pw):
    flat = tuple((v for pair in reversed(pw) for v in pair))
    return torch.nn.functional.pad(x, flat)
kernel_name = 'adaLN_cond__mha_block__glu_gated_glu__prompt'

def setup_inputs():

    _w = lambda a: (lambda t: t.to(torch.bfloat16) if t.is_floating_point() else t)(torch.as_tensor(a))

    def _A_setup():
        B, S, D, H = (1, 2048, 512, 32)
        return {'x': _w((lambda *s: torch.randn(*s, dtype=torch.bfloat16) * 0.05)(B, S, D)), 'wqkv': _w((lambda *s: torch.randn(*s, dtype=torch.bfloat16) * 0.05)(D, 3 * D)), 'wo': _w((lambda *s: torch.randn(*s, dtype=torch.bfloat16) * 0.05)(D, D))}
    _A_in = _A_setup()

    def _B_setup():
        B, S, D, F = (1, 2048, 512, 1376)
        return {'x': _w((lambda *s: torch.randn(*s, dtype=torch.bfloat16) * 0.05)(B, S, D)), 'w_gate': _w((lambda *s: torch.randn(*s, dtype=torch.bfloat16) * 0.05)(D, F)), 'w_up': _w((lambda *s: torch.randn(*s, dtype=torch.bfloat16) * 0.05)(D, F)), 'w_down': _w((lambda *s: torch.randn(*s, dtype=torch.bfloat16) * 0.05)(F, D))}
    _B_in = _B_setup()
    return {'A_x': _A_in['x'], 'A_wqkv': _A_in['wqkv'], 'A_wo': _A_in['wo'], 'B_x': _B_in['x'], 'B_w_gate': _B_in['w_gate'], 'B_w_up': _B_in['w_up'], 'B_w_down': _B_in['w_down'], 'c': _w((lambda *s: torch.randn(*s, dtype=torch.bfloat16) * 0.1)(1, 512) * 8.0), 'w_mod': _w((lambda *s: torch.randn(*s, dtype=torch.bfloat16) * 0.1)(512, 6 * 512) * 512 ** (-0.5) * 8.0)}

def reference(A_x, A_wqkv, A_wo, B_x, B_w_gate, B_w_up, B_w_down, c, w_mod):

    def _A_ref(x, wqkv, wo):
        B, S, D = x.shape
        H = 32
        Dh = D // H
        qkv = (lambda x, *d: x.permute(*d))((x @ wqkv).reshape(B, S, 3, H, Dh), 2, 0, 3, 1, 4)
        q, k, v = (qkv[0], qkv[1], qkv[2])
        scores = torch.einsum('bhqd,bhkd->bhqk', q, k) * Dh ** (-0.5)
        p = _softmax(scores, axis=-1)
        o = torch.einsum('bhqk,bhkd->bhqd', p, v)
        return (lambda x, *d: x.permute(*d))(o, 0, 2, 1, 3).reshape(B, S, D) @ wo

    def _B_ref(x, w_gate, w_up, w_down):
        g = torch.einsum('bsd,df->bsf', x, w_gate)
        u = torch.einsum('bsd,df->bsf', x, w_up)
        h = _sigmoid(g) * u
        return torch.einsum('bsf,fd->bsd', h, w_down)
    x = A_x
    mod = (_silu(c) @ w_mod).reshape(c.shape[0], 6, 1, -1)
    sa, ca, ga, sb, cb, gb = (mod[:, 0].clone(), mod[:, 1].clone(), mod[:, 2].clone(), mod[:, 3].clone(), mod[:, 4].clone(), mod[:, 5].clone())
    h = _layernorm(x, axis=-1) * (1 + ca) + sa
    x = x + ga * _A_ref(h, A_wqkv, A_wo)
    h = _layernorm(x, axis=-1) * (1 + cb) + sb
    x = x + gb * _B_ref(h, B_w_gate, B_w_up, B_w_down)
    return x
```

```cpp
#include <hip/hip_runtime.h>
#include <hip/hip_bf16.h>
#include <stdint.h>
#pragma clang diagnostic ignored "-Winline-asm"

typedef __attribute__((ext_vector_type(8))) __bf16 bf16x8;
typedef __attribute__((ext_vector_type(4))) short s16x4;
typedef __attribute__((ext_vector_type(8))) short s16x8;
typedef __attribute__((ext_vector_type(16))) float f32x16;
typedef __attribute__((ext_vector_type(4))) float f32x4;
typedef __attribute__((ext_vector_type(2))) float f32x2;
typedef __attribute__((ext_vector_type(4))) unsigned u32x4;
typedef __attribute__((ext_vector_type(2))) unsigned u32x2;
typedef unsigned short u16;
#define LDS_AS __attribute__((address_space(3)))
#define DI __device__ __forceinline__

constexpr int S_ = 2048, D_ = 512, H_ = 32, F_ = 1376;
constexpr int FB_ = F_ / 32;
constexpr int FK_ = F_ / 16;

constexpr size_t WS_MODB = 0;
constexpr size_t WS_ST1 = 8192;
constexpr size_t WS_KMAX = WS_ST1 + 16384;
constexpr size_t WS_UVP = WS_KMAX + 8192;
constexpr size_t WS_ST2 = WS_UVP + 45056;
constexpr size_t WS_Q = 1 << 20;
constexpr size_t WS_K = WS_Q + (2 << 20);
constexpr size_t WS_V = WS_K + (2 << 20);
constexpr size_t WS_O = WS_V + (2 << 20);
constexpr size_t WS_X1 = WS_O + (2 << 20);
constexpr size_t WS_Y = WS_X1 + (2 << 20);
constexpr size_t WS_HH = WS_Y + (2 << 20);

DI float asf(unsigned u) { return __builtin_bit_cast(float, u); }
DI float bf_lo(unsigned u) { return __builtin_bit_cast(float, u << 16); }
DI float bf_hi(unsigned u) { return __builtin_bit_cast(float, u & 0xffff0000u); }
DI float bf1(u16 u) { return __builtin_bit_cast(float, (unsigned)u << 16); }
typedef __attribute__((ext_vector_type(2))) __bf16 bf16x2;
DI unsigned pk2(float lo, float hi) {
  return __builtin_bit_cast(unsigned, __builtin_convertvector(f32x2{lo, hi}, bf16x2));
}
DI float bf_round(float x) { return bf_lo(pk2(x, x)); }

#define WAIT_V(n) asm volatile("s_waitcnt vmcnt(%0)" ::"n"(n) : "memory")
#define WAIT_L0() asm volatile("s_waitcnt lgkmcnt(0)" ::: "memory")
#define BAR() asm volatile("s_barrier" ::: "memory")

DI void glds16(const void* g, char* lds) {
  const unsigned a = (unsigned)(uintptr_t)(LDS_AS char*)lds;
  asm volatile("s_mov_b32 m0, %1\n\ts_nop 0\n\tglobal_load_lds_dwordx4 %0, off" ::"v"(g), "s"(a) : "m0", "memory");
}
DI void glds16s(const void* sbase, unsigned voff, char* lds) {
  const unsigned a = (unsigned)(uintptr_t)(LDS_AS char*)lds;
  asm volatile("s_mov_b32 m0, %2\n\ts_nop 0\n\tglobal_load_lds_dwordx4 %0, %1" ::"v"(voff), "s"(sbase), "s"(a)
               : "m0", "memory");
}
DI u32x4 aload16(const void* p) {
  u32x4 r;
  asm volatile("global_load_dwordx4 %0, %1, off" : "=v"(r) : "v"(p) : "memory");
  return r;
}
DI u32x2 aload8(const void* p) {
  u32x2 r;
  asm volatile("global_load_dwordx2 %0, %1, off" : "=v"(r) : "v"(p) : "memory");
  return r;
}
DI float aload4(const void* p) {
  float r;
  asm volatile("global_load_dword %0, %1, off" : "=v"(r) : "v"(p) : "memory");
  return r;
}
template <class T> DI void pin(T& v) { asm volatile("" : "+v"(v)); }

DI bf16x8 lds_frag(const char* p) { return *(const bf16x8*)p; }
typedef __attribute__((ext_vector_type(4))) short v4i16_t;
DI bf16x8 tr_frag(const char* p0, const char* p1) {
  v4i16_t a = __builtin_amdgcn_ds_read_tr16_b64_v4i16((LDS_AS v4i16_t*)p0);
  v4i16_t b = __builtin_amdgcn_ds_read_tr16_b64_v4i16((LDS_AS v4i16_t*)p1);
  s16x8 r = __builtin_shufflevector(a, b, 0, 1, 2, 3, 4, 5, 6, 7);
  return __builtin_bit_cast(bf16x8, r);
}
DI f32x16 mfma(bf16x8 a, bf16x8 b, f32x16 c) { return __builtin_amdgcn_mfma_f32_32x32x16_bf16(a, b, c, 0, 0, 0); }

template <int CTRL, int RM> DI float dppf(float v) {
  return __builtin_bit_cast(float, __builtin_amdgcn_update_dpp(0, __builtin_bit_cast(int, v), CTRL, RM, 0xf, true));
}
DI float wave_sum(float v) {
  v += dppf<0x111, 0xf>(v);
  v += dppf<0x112, 0xf>(v);
  v += dppf<0x114, 0xf>(v);
  v += dppf<0x118, 0xf>(v);
  v += dppf<0x142, 0xa>(v);
  v += dppf<0x143, 0xc>(v);
  return __builtin_bit_cast(float, __builtin_amdgcn_readlane(__builtin_bit_cast(int, v), 63));
}
DI float wave_max_nonneg(float v) {
  v = fmaxf(v, dppf<0x111, 0xf>(v));
  v = fmaxf(v, dppf<0x112, 0xf>(v));
  v = fmaxf(v, dppf<0x114, 0xf>(v));
  v = fmaxf(v, dppf<0x118, 0xf>(v));
  v = fmaxf(v, dppf<0x142, 0xa>(v));
  v = fmaxf(v, dppf<0x143, 0xc>(v));
  return __builtin_bit_cast(float, __builtin_amdgcn_readlane(__builtin_bit_cast(int, v), 63));
}
DI float other_half(float v) {
  unsigned u = __builtin_bit_cast(unsigned, v);
  auto r = __builtin_amdgcn_permlane32_swap(u, u, false, false);
  unsigned lo = r[0], hi = r[1];
  return __builtin_bit_cast(float, (__lane_id() < 32) ? hi : lo);
}

__global__ __launch_bounds__(512) void k0_mod(const u16* __restrict__ c, const u16* __restrict__ wmod,
                                              const u16* __restrict__ x, u16* __restrict__ modb,
                                              float2* __restrict__ st1) {
  __shared__ float red[128 * 36];
  __shared__ float red2[16 * 32];
  const int b = blockIdx.x, t = threadIdx.x;
  if (b < 96) {
    const int ch = t & 3, r = t >> 2;
    const u16* wp = wmod + (size_t)r * 3072 + b * 32 + ch * 8;
    u32x4 w[4];
#pragma unroll
    for (int i = 0; i < 4; i++) w[i] = *(const u32x4*)(wp + (size_t)i * 128 * 3072);
    float acc[8] = {0, 0, 0, 0, 0, 0, 0, 0};
#pragma unroll
    for (int i = 0; i < 4; i++) {
      float cv = bf1(c[r + 128 * i]);
      float s = bf_round(cv / (1.0f + __expf(-cv)));
#pragma unroll
      for (int j = 0; j < 4; j++) {
        acc[2 * j] += s * bf_lo(w[i][j]);
        acc[2 * j + 1] += s * bf_hi(w[i][j]);
      }
    }
    *(f32x4*)&red[r * 36 + ch * 8] = f32x4{acc[0], acc[1], acc[2], acc[3]};
    *(f32x4*)&red[r * 36 + ch * 8 + 4] = f32x4{acc[4], acc[5], acc[6], acc[7]};
    __syncthreads();
    {
      const int col = t & 31, part = t >> 5;
      float s = 0;
#pragma unroll
      for (int i = 0; i < 8; i++) s += red[(part * 8 + i) * 36 + col];
      red2[part * 32 + col] = s;
    }
    __syncthreads();
    if (t < 32) {
      float s = 0;
#pragma unroll
      for (int i = 0; i < 16; i++) s += red2[i * 32 + t];
      const int col = b * 32 + t, chunk = col >> 9;
      s = bf_round(s);
      if (chunk == 1 || chunk == 4) s = 1.0f + s;
      modb[col] = (u16)(pk2(s, s) & 0xffffu);
    }
  } else {
    const int w = t >> 6, lane = t & 63;
    const int row0 = (b - 96) * 16 + w * 2;
    u32x4 v[2];
#pragma unroll
    for (int i = 0; i < 2; i++) v[i] = *(const u32x4*)(x + (size_t)(row0 + i) * D_ + lane * 8);
#pragma unroll
    for (int i = 0; i < 2; i++) {
      float e[8];
#pragma unroll
      for (int j = 0; j < 4; j++) { e[2 * j] = bf_lo(v[i][j]); e[2 * j + 1] = bf_hi(v[i][j]); }
      float s = 0;
#pragma unroll
      for (int j = 0; j < 8; j++) s += e[j];
      const float mean = wave_sum(s) * (1.0f / D_);
      float q = 0;
#pragma unroll
      for (int j = 0; j < 8; j++) { float d = e[j] - mean; q += d * d; }
      const float var = wave_sum(q) * (1.0f / D_);
      const float rstd = 1.0f / sqrtf(var + 1e-5f);
      if (lane == 0) st1[row0 + i] = make_float2(rstd, -mean * rstd);
    }
  }
}

__global__ __launch_bounds__(256, 1) void k1_qkv(const u16* __restrict__ x, const u16* __restrict__ wqkv,
                                                 const u16* __restrict__ wg, const u16* __restrict__ wu,
                                                 const u16* __restrict__ modb, const float2* __restrict__ st1,
                                                 u16* __restrict__ qf, u16* __restrict__ kf, u16* __restrict__ vf,
                                                 float* __restrict__ kmax, float* __restrict__ uvp) {
  __shared__ __attribute__((aligned(1024))) char smem[163840];
  constexpr int RING = 65536, SLOT = 24576;
  const int t = threadIdx.x, lane = t & 63;
  const int w = __builtin_amdgcn_readfirstlane(t >> 6);
  const int xcd = blockIdx.x & 7, loc = blockIdx.x >> 3;
  const int rb = (xcd >> 1) * 8 + (loc & 7), cg = (xcd & 1) * 4 + (loc >> 3);
  const int row0 = rb * 64;

  u32x4 mB = aload16(modb + lane * 8);
  u32x4 mA = aload16(modb + 512 + lane * 8);
#pragma unroll
  for (int i = 0; i < 16; i++) {
    const int r = w * 16 + i;
    glds16(x + (size_t)(row0 + r) * D_ + ((lane ^ i) << 3), smem + r * 1024);
  }
  const int krow = (lane & 31) >> 2, nch = lane & 3, hb = lane >> 5;
  const u16* wsrc_qk = wqkv + (size_t)(w * 16 + krow) * 1536 + cg * 128 + hb * 32 + nch * 8;
  const u16* wsrc_v = wqkv + (size_t)(w * 16 + krow) * 1536 + 1024 + cg * 64 + hb * 32 + nch * 8;
#pragma unroll
  for (int c = 0; c < 4; c++) {
    char* dst = smem + RING + c * SLOT + w * 6144;
#pragma unroll
    for (int i = 0; i < 6; i++) {
      const size_t ro = (size_t)(c * 64 + (i / 3) * 8) * 1536;
      const u16* src = ((i % 3) == 2) ? wsrc_v + ro : wsrc_qk + ro + (i % 3) * 64;
      glds16(src, dst + i * 1024);
    }
  }
  WAIT_V(24);
  pin(mA); pin(mB);
  {
    float A[8], B[8];
#pragma unroll
    for (int j = 0; j < 4; j++) {
      A[2 * j] = bf_lo(mA[j]); A[2 * j + 1] = bf_hi(mA[j]);
      B[2 * j] = bf_lo(mB[j]); B[2 * j + 1] = bf_hi(mB[j]);
    }
#pragma unroll
    for (int i = 0; i < 16; i++) {
      const int r = w * 16 + i;
      const float2 st = st1[row0 + r];
      char* p = smem + r * 1024 + ((lane ^ i) << 4);
      u32x4 v = *(const u32x4*)p;
      u32x4 o;
#pragma unroll
      for (int j = 0; j < 4; j++) {
        float lo = (bf_lo(v[j]) * st.x + st.y) * A[2 * j] + B[2 * j];
        float hi = (bf_hi(v[j]) * st.x + st.y) * A[2 * j + 1] + B[2 * j + 1];
        o[j] = pk2(lo, hi);
      }
      *(u32x4*)p = o;
    }
  }
  f32x16 acc[3];
#pragma unroll
  for (int n = 0; n < 3; n++)
#pragma unroll
    for (int r = 0; r < 16; r++) acc[n][r] = 0.f;
  const int th = w & 1, role = w >> 1;
  const int hh = lane >> 5, r31 = lane & 31, r15 = lane & 15;
  {
    int aoff[8];
#pragma unroll
    for (int b = 0; b < 8; b++) aoff[b] = (th * 32 + r31) * 1024 + (((2 * b + hh) ^ r15) << 4);
    const int woff = hh * 3072 + (r15 >> 2) * 64 + ((lane >> 4) & 1) * 32 + (lane & 3) * 8 + role * 3 * 512;
#define K1_LOOP(ROLE)                                                                                       \
  _Pragma("unroll") for (int c = 0; c < 8; c++) {                                                           \
    if (c == 0) WAIT_V(18); else if (c <= 5) WAIT_V(12); else if (c == 6) WAIT_V(6); else WAIT_V(0);        \
    WAIT_L0();                                                                                              \
    BAR();                                                                                                  \
    if (c >= 1 && c + 3 < 8) {                                                                              \
      char* dst = smem + RING + ((c + 3) & 3) * SLOT + w * 6144;                                            \
      _Pragma("unroll") for (int i = 0; i < 6; i++) {                                                       \
        const size_t ro = (size_t)((c + 3) * 64 + (i / 3) * 8) * 1536;                                      \
        const u16* src = ((i % 3) == 2) ? wsrc_v + ro : wsrc_qk + ro + (i % 3) * 64;                        \
        glds16(src, dst + i * 1024);                                                                        \
      }                                                                                                     \
    }                                                                                                       \
    const char* ws = smem + RING + (c & 3) * SLOT + woff;                                                   \
    _Pragma("unroll") for (int s = 0; s < 4; s++) {                                                         \
      const int ks = c * 4 + s;                                                                             \
      bf16x8 a = lds_frag(smem + aoff[ks & 7] + (ks >> 3) * 256);                                           \
      _Pragma("unroll") for (int n = 0; n < 3; n++) {                                                       \
        bf16x8 wf = tr_frag(ws + s * 6144 + n * 512, ws + s * 6144 + n * 512 + 256);                        \
        if (ROLE == 1 && n >= 1) acc[n] = mfma(a, wf, acc[n]);                                              \
        else acc[n] = mfma(wf, a, acc[n]);                                                                  \
      }                                                                                                     \
    }                                                                                                       \
  }
    if (role == 0) { K1_LOOP(0) } else { K1_LOOP(1) }
#undef K1_LOOP
  }
  const int tb = rb * 2 + th;
#pragma unroll
  for (int n = 0; n < 3; n++) {
    const int nb = role * 3 + n;
    if (nb < 4) {
      const int blk = cg * 4 + nb;
      const bool isq = blk < 16;
      const float sc = isq ? 0.25f * 1.4426950408889634f : 1.0f;
      u16* dst = isq ? qf : kf;
#pragma unroll
      for (int e = 0; e < 2; e++) {
        const int head = (blk & 15) * 2 + e;
        u32x4 o;
        float n2 = 0;
#pragma unroll
        for (int j = 0; j < 4; j++) {
          float lo = acc[n][8 * e + 2 * j] * sc, hi = acc[n][8 * e + 2 * j + 1] * sc;
          n2 += lo * lo + hi * hi;
          o[j] = pk2(lo, hi);
        }
        *(u32x4*)(dst + ((size_t)(head * 64 + tb) * 64 + lane) * 8) = o;
        if (!isq) {
          n2 += other_half(n2);
          n2 = wave_max_nonneg(n2);
          if (lane == 0) kmax[head * 64 + tb] = n2;
        }
      }
    } else {
      const int head = (cg * 2 + (nb - 4)) * 2 + ((lane >> 4) & 1);
#pragma unroll
      for (int e = 0; e < 2; e++) {
        u32x4 o;
#pragma unroll
        for (int j = 0; j < 4; j++) o[j] = pk2(acc[n][8 * e + 2 * j], acc[n][8 * e + 2 * j + 1]);
        *(u32x4*)(vf + ((size_t)(head * 128 + tb * 2 + e) * 32 + hh * 16 + r15) * 8) = o;
      }
    }
  }
  if (blockIdx.x < 172) {
    const int g = blockIdx.x % 86, kh = blockIdx.x / 86;
    const u16* wm = (g < FB_) ? wg + g * 32 : wu + (g - FB_) * 32;
    const int ch = t & 3, r = t >> 2;
    u32x4 wv[4];
    float a2[4], sb[4];
#pragma unroll
    for (int i = 0; i < 4; i++) {
      const int k = kh * 256 + r + 64 * i;
      wv[i] = *(const u32x4*)(wm + (size_t)k * F_ + ch * 8);
      sb[i] = bf1(modb[1536 + k]);
      a2[i] = bf1(modb[2048 + k]);
    }
    float au[8] = {0, 0, 0, 0, 0, 0, 0, 0}, at[8] = {0, 0, 0, 0, 0, 0, 0, 0};
#pragma unroll
    for (int i = 0; i < 4; i++)
#pragma unroll
      for (int j = 0; j < 4; j++) {
        const float lo = bf_lo(wv[i][j]), hi = bf_hi(wv[i][j]);
        au[2 * j] += a2[i] * lo; au[2 * j + 1] += a2[i] * hi;
        at[2 * j] += sb[i] * lo; at[2 * j + 1] += sb[i] * hi;
      }
    __syncthreads();
    float* red = (float*)smem;
    *(f32x4*)&red[r * 68 + ch * 8] = f32x4{au[0], au[1], au[2], au[3]};
    *(f32x4*)&red[r * 68 + ch * 8 + 4] = f32x4{au[4], au[5], au[6], au[7]};
    *(f32x4*)&red[r * 68 + 32 + ch * 8] = f32x4{at[0], at[1], at[2], at[3]};
    *(f32x4*)&red[r * 68 + 32 + ch * 8 + 4] = f32x4{at[4], at[5], at[6], at[7]};
    __syncthreads();
    float* red2 = red + 64 * 68;
    {
      const int col = t & 63, part = t >> 6;
      float s = 0;
#pragma unroll
      for (int i = 0; i < 16; i++) s += red[(part * 16 + i) * 68 + col];
      red2[part * 64 + col] = s;
    }
    __syncthreads();
    if (t < 64) {
      const float s = red2[t] + red2[64 + t] + red2[128 + t] + red2[192 + t];
      uvp[(kh * 2 + (t >> 5)) * 2752 + g * 32 + (t & 31)] = s;
    }
  }
}

constexpr int A_K = 0, A_V = 65536, A_ONES = 131072;
DI void attn_tiles(const char* smem, int kaddr, int vaddr, int vstep, int t0, int t1, const bf16x8 qf, const f32x16& negm,
                   f32x16& o) {
  kaddr += t0 * 1024;
  vaddr += (t0 >> 2) * vstep;
  for (int t = t0; t < t1; t += 4) {
#pragma unroll
    for (int u = 0; u < 4; u++) {
      bf16x8 kfr = lds_frag(smem + kaddr + u * 1024);
      bf16x8 v0 = lds_frag(smem + vaddr + u * 1024);
      bf16x8 v1 = lds_frag(smem + vaddr + u * 1024 + 512);
      f32x16 s = mfma(kfr, qf, negm);
      u32x4 p0, p1;
#pragma unroll
      for (int j = 0; j < 4; j++) {
        p0[j] = pk2(__builtin_amdgcn_exp2f(s[2 * j]), __builtin_amdgcn_exp2f(s[2 * j + 1]));
        p1[j] = pk2(__builtin_amdgcn_exp2f(s[8 + 2 * j]), __builtin_amdgcn_exp2f(s[8 + 2 * j + 1]));
      }
      o = mfma(v0, __builtin_bit_cast(bf16x8, p0), o);
      o = mfma(v1, __builtin_bit_cast(bf16x8, p1), o);
    }
    kaddr += 4096;
    vaddr += vstep;
  }
}

__global__ __launch_bounds__(512, 2) void k2_attn(const u16* __restrict__ qf, const u16* __restrict__ kf,
                                                  const u16* __restrict__ vf, const float* __restrict__ kmax,
                                                  u16* __restrict__ of) {
  __shared__ __attribute__((aligned(1024))) char smem[131072 + 4096];
  const int t = threadIdx.x, lane = t & 63;
  const int w = __builtin_amdgcn_readfirstlane(t >> 6);
  const int head = blockIdx.x & 31, qg = blockIdx.x >> 5;
  const int tb = qg * 8 + w;
  u32x4 qraw = aload16(qf + ((size_t)(head * 64 + tb) * 64 + lane) * 8);
  float km = aload4(kmax + head * 64 + lane);
  const char* kg = (const char*)(kf + (size_t)head * 32768) + lane * 16;
  const char* vg = (const char*)(vf + (size_t)head * 32768) + lane * 16;
#pragma unroll
  for (int c = 0; c < 8; c++) {
    glds16(kg + (c * 8 + w) * 1024, smem + A_K + (c * 8 + w) * 1024);
    glds16(vg + (c * 8 + w) * 1024, smem + A_V + (c * 8 + w) * 1024);
  }
  if (t < 256) *(u32x4*)(smem + A_ONES + t * 16) = u32x4{0x3f803f80u, 0x3f803f80u, 0x3f803f80u, 0x3f803f80u};
  WAIT_V(14);
  pin(qraw); pin(km);
  float q2 = 0;
#pragma unroll
  for (int j = 0; j < 4; j++) { float lo = bf_lo(qraw[j]), hi = bf_hi(qraw[j]); q2 += lo * lo + hi * hi; }
  q2 += other_half(q2);
  const float k2 = wave_max_nonneg(km);
  const float mb = sqrtf(q2 * k2) * 1.01f;
  const bf16x8 qfr = __builtin_bit_cast(bf16x8, qraw);
  f32x16 negm, o;
#pragma unroll
  for (int r = 0; r < 16; r++) { negm[r] = -mb; o[r] = 0.f; }
  const int kaddr = A_K + lane * 16;
  const bool isv = (lane & 16) == 0;
  const int vaddr = isv ? A_V + ((lane >> 5) * 16 + (lane & 15)) * 16 : A_ONES;
  const int vstep = isv ? 4096 : 0;
  WAIT_L0();
  BAR();
  attn_tiles(smem, kaddr, vaddr, vstep, 0, 8, qfr, negm, o);
  WAIT_V(0);
  BAR();
  attn_tiles(smem, kaddr, vaddr, vstep, 8, 64, qfr, negm, o);
  if (__builtin_amdgcn_ballot_w64(!(o[8] >= 5.4e-20f)) != 0) {
    float m = -INFINITY;
    f32x16 z;
#pragma unroll
    for (int r = 0; r < 16; r++) z[r] = 0.f;
    for (int tt = 0; tt < 64; tt++) {
      f32x16 s = mfma(lds_frag(smem + kaddr + tt * 1024), qfr, z);
#pragma unroll
      for (int r = 0; r < 16; r++) m = fmaxf(m, s[r]);
    }
    m = fmaxf(m, other_half(m));
#pragma unroll
    for (int r = 0; r < 16; r++) { negm[r] = -m; o[r] = 0.f; }
    attn_tiles(smem, kaddr, vaddr, vstep, 0, 64, qfr, negm, o);
  }
  const float inv = 1.0f / o[8];
  u32x4 r;
#pragma unroll
  for (int j = 0; j < 4; j++) r[j] = pk2(o[2 * j] * inv, o[2 * j + 1] * inv);
  *(u32x4*)(of + ((size_t)(tb * 32 + head) * 64 + lane) * 8) = r;
}

__global__ __launch_bounds__(256, 1) void k3_oproj(const u16* __restrict__ of, const u16* __restrict__ wo,
                                                   const u16* __restrict__ x, const u16* __restrict__ modb,
                                                   u16* __restrict__ x1f, u16* __restrict__ yf,
                                                   float2* __restrict__ st2) {
  __shared__ __attribute__((aligned(1024))) char smem[131072];
  const int t = threadIdx.x, lane = t & 63;
  const int w = __builtin_amdgcn_readfirstlane(t >> 6);
  const int xcd = blockIdx.x & 7, loc = blockIdx.x >> 3;
  const int rb = xcd * 4 + (loc & 3), cg = loc >> 2;
  const int th = w & 1, nh = w >> 1, hh = lane >> 5, r31 = lane & 31, r15 = lane & 15;
  const int tb = rb * 2 + th, n0 = cg * 64 + nh * 32;
  u32x2 xv[4], gv[4], av[4];
#pragma unroll
  for (int g = 0; g < 4; g++) {
    const int col = n0 + g * 8 + hh * 4;
    xv[g] = aload8(x + (size_t)(tb * 32 + r31) * D_ + col);
    gv[g] = aload8(modb + 1024 + col);
    av[g] = aload8(modb + 2048 + col);
  }
  const u16* asrc = of + ((size_t)(rb * 2 + (w >> 1)) * 32 + (w & 1) * 2) * 512 + lane * 8;
  const u16* wsrc = wo + (size_t)(w * 16 + ((lane & 31) >> 2)) * D_ + cg * 64 + (lane >> 5) * 32 + (lane & 3) * 8;
#pragma unroll
  for (int c = 0; c < 8; c++) {
    char* dst = smem + c * 16384;
    glds16(asrc + c * 4 * 512, dst + w * 2048);
    glds16(asrc + c * 4 * 512 + 512, dst + w * 2048 + 1024);
    glds16(wsrc + (size_t)c * 64 * D_, dst + 8192 + w * 2048);
    glds16(wsrc + (size_t)(c * 64 + 8) * D_, dst + 8192 + w * 2048 + 1024);
  }
  f32x16 acc[2];
#pragma unroll
  for (int r = 0; r < 16; r++) acc[0][r] = acc[1][r] = 0.f;
  const int aoff = th * 4096 + lane * 16;
  const int woff = 8192 + nh * 512 + (hh * 4 + (r15 >> 2)) * 64 + ((lane >> 4) & 1) * 32 + (lane & 3) * 8;
#pragma unroll
  for (int c = 0; c < 8; c++) {
    WAIT_V(4 * (7 - c));
    BAR();
    const char* base = smem + c * 16384;
#pragma unroll
    for (int s = 0; s < 4; s++) {
      bf16x8 a = lds_frag(base + aoff + s * 1024);
      bf16x8 wf = tr_frag(base + woff + s * 2048, base + woff + s * 2048 + 1024);
      acc[s & 1] = mfma(wf, a, acc[s & 1]);
    }
  }
#pragma unroll
  for (int g = 0; g < 4; g++) { pin(xv[g]); pin(gv[g]); pin(av[g]); }
  float x1[16];
  float sum = 0;
#pragma unroll
  for (int g = 0; g < 4; g++)
#pragma unroll
    for (int j = 0; j < 2; j++) {
      const int r = g * 4 + j * 2;
      float lo = bf_lo(xv[g][j]) + bf_lo(gv[g][j]) * (acc[0][r] + acc[1][r]);
      float hi = bf_hi(xv[g][j]) + bf_hi(gv[g][j]) * (acc[0][r + 1] + acc[1][r + 1]);
      const unsigned p = pk2(lo, hi);
      xv[g][j] = p;
      x1[r] = bf_lo(p); x1[r + 1] = bf_hi(p);
      sum += x1[r] + x1[r + 1];
    }
  sum += other_half(sum);
  const float mean = sum * (1.0f / 32.0f);
  float m2 = 0;
#pragma unroll
  for (int r = 0; r < 16; r++) { const float d = x1[r] - mean; m2 += d * d; }
  m2 += other_half(m2);
  if (lane < 32) st2[(size_t)(tb * 32 + r31) * 16 + cg * 2 + nh] = make_float2(mean, m2);
#pragma unroll
  for (int e = 0; e < 2; e++) {
    const size_t o = ((size_t)(tb * 32 + cg * 4 + nh * 2 + e) * 64 + lane) * 8;
    *(u32x4*)(x1f + o) = u32x4{xv[2 * e][0], xv[2 * e][1], xv[2 * e + 1][0], xv[2 * e + 1][1]};
    u32x4 y;
#pragma unroll
    for (int j = 0; j < 4; j++) {
      const int g = 2 * e + (j >> 1), jj = j & 1, r = g * 4 + jj * 2;
      y[j] = pk2(x1[r] * bf_lo(av[g][jj]), x1[r + 1] * bf_hi(av[g][jj]));
    }
    *(u32x4*)(yf + o) = y;
  }
}

__global__ __launch_bounds__(256, 1) void k4_gateup(const u16* __restrict__ yf, const u16* __restrict__ wg,
                                                    const u16* __restrict__ wu, const float* __restrict__ uvp,
                                                    const float2* __restrict__ st2, u16* __restrict__ hf) {
  constexpr int SLOT = 20480, R = 7, XTRA = SLOT * R;
  __shared__ __attribute__((aligned(1024))) char smem[XTRA + 2048];
  const int t = threadIdx.x, lane = t & 63;
  const int w = __builtin_amdgcn_readfirstlane(t >> 6);
  const int xcd = blockIdx.x & 7, loc = blockIdx.x >> 3;
  const int rb = xcd * 2 + (loc & 1), cg = loc >> 1;
  const int hh = lane >> 5, r31 = lane & 31, r15 = lane & 15;
  const int tb = rb * 4 + w;
  u32x4 sraw[4];
#pragma unroll
  for (int i = 0; i < 4; i++) sraw[i] = aload16((const char*)(st2 + (size_t)(tb * 32 + r31) * 16 + hh * 8) + i * 16);
  float uv0 = 0, uv1 = 0, uv2 = 0, uv3 = 0;
  {
    const int tt = t < 192 ? t : 0;
    const int nb = tt >> 5;
    const int fb = min(cg * 3 + (nb % 3), FB_ - 1);
    const int col = (nb / 3) * F_ + fb * 32 + (tt & 31);
    uv0 = aload4(uvp + col); uv1 = aload4(uvp + 2752 + col); uv2 = aload4(uvp + 2 * 2752 + col); uv3 = aload4(uvp + 3 * 2752 + col);
  }
  const u16* asrc = yf + (size_t)tb * 32 * 512 + lane * 8;
  const u16* wsrc[3];
#pragma unroll
  for (int i = 0; i < 3; i++) {
    const int nb = 2 * i + hh;
    const int fb = min(cg * 3 + (nb % 3), FB_ - 1);
    wsrc[i] = (nb < 3 ? wg : wu) + (size_t)(w * 8 + (r31 >> 2)) * F_ + fb * 32 + (lane & 3) * 8;
  }
#define K4_ISSUE(c)                                                                     \
  {                                                                                     \
    char* dst = smem + ((c) % R) * SLOT;                                                \
    glds16(asrc + (c) * 1024, dst + w * 2048);                                          \
    glds16(asrc + (c) * 1024 + 512, dst + w * 2048 + 1024);                             \
    _Pragma("unroll") for (int i = 0; i < 3; i++)                                       \
        glds16(wsrc[i] + (size_t)(c) * 32 * F_, dst + 8192 + w * 3072 + i * 1024);      \
  }
#pragma unroll
  for (int c = 0; c < R; c++) K4_ISSUE(c)
  f32x16 acc[6];
#pragma unroll
  for (int n = 0; n < 6; n++)
#pragma unroll
    for (int r = 0; r < 16; r++) acc[n][r] = 0.f;
  const int aoff = w * 2048 + lane * 16;
  const int woff = 8192 + (hh * 4 + (r15 >> 2)) * 64 + ((lane >> 4) & 1) * 32 + (lane & 3) * 8;
#pragma unroll
  for (int c = 0; c < 16; c++) {
    const int hi = (c == 0) ? R - 1 : (c + R - 2 < 15 ? c + R - 2 : 15);
    WAIT_V(5 * (hi - c));
    if (c == 0) {
      pin(uv0); pin(uv1); pin(uv2); pin(uv3);
      if (t < 192) {
        float* xs = (float*)(smem + XTRA);
        xs[t] = uv0 + uv2;
        xs[192 + t] = uv1 + uv3;
      }
    }
    WAIT_L0();
    BAR();
    if (c >= 1 && c + R - 1 < 16) K4_ISSUE(c + R - 1)
    const char* base = smem + (c % R) * SLOT;
#pragma unroll
    for (int s = 0; s < 2; s++) {
      bf16x8 a = lds_frag(base + aoff + s * 1024);
#pragma unroll
      for (int n = 0; n < 6; n++) {
        bf16x8 wf = tr_frag(base + woff + s * 6144 + n * 512, base + woff + s * 6144 + 3072 + n * 512);
        acc[n] = mfma(wf, a, acc[n]);
      }
    }
  }
#undef K4_ISSUE
#pragma unroll
  for (int i = 0; i < 4; i++) pin(sraw[i]);
  float msum = 0, m2 = 0;
#pragma unroll
  for (int i = 0; i < 4; i++) {
    msum += asf(sraw[i][0]) + asf(sraw[i][2]);
    m2 += asf(sraw[i][1]) + asf(sraw[i][3]);
  }
  msum += other_half(msum);
  const float mean = msum * (1.0f / 16.0f);
#pragma unroll
  for (int i = 0; i < 4; i++) {
    const float d0 = asf(sraw[i][0]) - mean, d1 = asf(sraw[i][2]) - mean;
    m2 += 32.0f * (d0 * d0 + d1 * d1);
  }
  m2 += other_half(m2);
  const float rstd = 1.0f / sqrtf(m2 * (1.0f / D_) + 1e-5f);
  const float nm = -mean * rstd;
  const float* xs = (const float*)(smem + XTRA);
#pragma unroll
  for (int n = 0; n < 3; n++) {
    const int fb = cg * 3 + n;
    if (fb < FB_) {
      unsigned pk[8];
#pragma unroll
      for (int g = 0; g < 4; g++) {
        const int cidx = n * 32 + g * 8 + hh * 4;
        const f32x4 ug = *(const f32x4*)(xs + cidx), tg = *(const f32x4*)(xs + 192 + cidx);
        const f32x4 uu = *(const f32x4*)(xs + 96 + cidx), tu = *(const f32x4*)(xs + 192 + 96 + cidx);
        float hv[4];
#pragma unroll
        for (int j = 0; j < 4; j++) {
          const float gt = acc[n][g * 4 + j] * rstd + (nm * ug[j] + tg[j]);
          const float up = acc[n + 3][g * 4 + j] * rstd + (nm * uu[j] + tu[j]);
          hv[j] = up * __builtin_amdgcn_rcpf(1.0f + __builtin_amdgcn_exp2f(-1.4426950408889634f * gt));
        }
        pk[2 * g] = pk2(hv[0], hv[1]);
        pk[2 * g + 1] = pk2(hv[2], hv[3]);
      }
#pragma unroll
      for (int e = 0; e < 2; e++)
        *(u32x4*)(hf + ((size_t)(tb * FK_ + fb * 2 + e) * 64 + lane) * 8) =
            u32x4{pk[4 * e], pk[4 * e + 1], pk[4 * e + 2], pk[4 * e + 3]};
    }
  }
}

__global__ __launch_bounds__(256, 1) void k5_down(const u16* __restrict__ hf, const u16* __restrict__ wd,
                                                  const u16* __restrict__ x1f, const u16* __restrict__ modb,
                                                  u16* __restrict__ out) {
  constexpr int SLOT = 32768, R = 5;
  __shared__ __attribute__((aligned(1024))) char smem[SLOT * R];
  const int t = threadIdx.x, lane = t & 63;
  const int w = __builtin_amdgcn_readfirstlane(t >> 6);
  const int xcd = blockIdx.x & 7, loc = blockIdx.x >> 3;
  const int rb = xcd * 4 + (loc & 3), cg = loc >> 2;
  const int th = w & 1, nh = w >> 1, hh = lane >> 5, r31 = lane & 31, r15 = lane & 15;
  const int tb = rb * 2 + th, n0 = cg * 64 + nh * 32;
  u32x4 xv[2];
  u32x2 gv[4];
#pragma unroll
  for (int e = 0; e < 2; e++) xv[e] = aload16(x1f + ((size_t)(tb * 32 + cg * 4 + nh * 2 + e) * 64 + lane) * 8);
#pragma unroll
  for (int g = 0; g < 4; g++) gv[g] = aload8(modb + 2560 + n0 + g * 8 + hh * 4);
  const u16* asrc = hf + (size_t)(rb * 2 + (w >> 1)) * FK_ * 512 + lane * 8;
  const u16* wsrc = wd + (size_t)(r31 >> 2) * D_ + cg * 64 + hh * 32 + (lane & 3) * 8;
#define K5_ISSUE(c)                                                                                    \
  {                                                                                                    \
    char* dst = smem + ((c) % R) * SLOT;                                                               \
    constexpr int NP = ((c) == 10) ? 3 : 4;                                                            \
    _Pragma("unroll") for (int i = 0; i < NP; i++) {                                                   \
      const int ks = (w & 1) * NP + i;                                                                 \
      glds16(asrc + ((c) * 8 + ks) * 512, dst + (w >> 1) * 8192 + ks * 1024);                          \
    }                                                                                                  \
    _Pragma("unroll") for (int i = 0; i < NP; i++) {                                                   \
      const int kb = w * NP + i;                                                                       \
      glds16(wsrc + (size_t)((c) * 128 + kb * 8) * D_, dst + 16384 + kb * 1024);                       \
    }                                                                                                  \
  }
  K5_ISSUE(0) K5_ISSUE(1) K5_ISSUE(2) K5_ISSUE(3) K5_ISSUE(4)
  f32x16 acc[2];
#pragma unroll
  for (int r = 0; r < 16; r++) acc[0][r] = acc[1][r] = 0.f;
  const int aoff = th * 8192 + lane * 16;
  const int woff = 16384 + nh * 512 + (hh * 4 + (r15 >> 2)) * 64 + ((lane >> 4) & 1) * 32 + (lane & 3) * 8;
#define K5_STEP(c, nwait, nissue)                                                                      \
  {                                                                                                    \
    WAIT_V(nwait);                                                                                     \
    WAIT_L0();                                                                                         \
    BAR();                                                                                             \
    if (nissue >= 0) K5_ISSUE((nissue >= 0 ? nissue : 0))                                              \
    const char* base = smem + ((c) % R) * SLOT;                                                        \
    _Pragma("unroll") for (int s = 0; s < ((c) == 10 ? 6 : 8); s++) {                                  \
      bf16x8 a = lds_frag(base + aoff + s * 1024);                                                     \
      bf16x8 wf = tr_frag(base + woff + s * 2048, base + woff + s * 2048 + 1024);                      \
      acc[s & 1] = mfma(wf, a, acc[s & 1]);                                                            \
    }                                                                                                  \
  }
  K5_STEP(0, 32, -1)
  K5_STEP(1, 24, 5)
  K5_STEP(2, 24, 6)
  K5_STEP(3, 24, 7)
  K5_STEP(4, 24, 8)
  K5_STEP(5, 24, 9)
  K5_STEP(6, 24, 10)
  K5_STEP(7, 22, -1)
  K5_STEP(8, 14, -1)
  K5_STEP(9, 6, -1)
  K5_STEP(10, 0, -1)
#undef K5_STEP
#undef K5_ISSUE
  pin(xv[0]); pin(xv[1]);
#pragma unroll
  for (int g = 0; g < 4; g++) pin(gv[g]);
  unsigned pk[8];
#pragma unroll
  for (int g = 0; g < 4; g++)
#pragma unroll
    for (int j = 0; j < 2; j++) {
      const int r = g * 4 + j * 2;
      const unsigned xr = xv[g >> 1][(g & 1) * 2 + j];
      const float lo = bf_lo(xr) + bf_lo(gv[g][j]) * (acc[0][r] + acc[1][r]);
      const float hi = bf_hi(xr) + bf_hi(gv[g][j]) * (acc[0][r + 1] + acc[1][r + 1]);
      pk[g * 2 + j] = pk2(lo, hi);
    }
  u16* orow = out + (size_t)(tb * 32 + r31) * D_ + n0;
#pragma unroll
  for (int e = 0; e < 2; e++) {
    auto r0 = __builtin_amdgcn_permlane32_swap(pk[4 * e], pk[4 * e + 2], false, false);
    auto r1 = __builtin_amdgcn_permlane32_swap(pk[4 * e + 1], pk[4 * e + 3], false, false);
    const u32x4 v = u32x4{r0[0], r1[0], r0[1], r1[1]};
    *(u32x4*)(orow + e * 16 + hh * 8) = v;
  }
}

extern "C" void kernel_launch(void* const* d_in, const int* in_sizes, int n_in, void* d_out, int out_size, void* d_ws,
                              size_t ws_size, hipStream_t stream) {
  const u16* x = (const u16*)d_in[0];
  const u16* wqkv = (const u16*)d_in[1];
  const u16* wo = (const u16*)d_in[2];
  const u16* wg = (const u16*)d_in[4];
  const u16* wu = (const u16*)d_in[5];
  const u16* wd = (const u16*)d_in[6];
  const u16* c = (const u16*)d_in[7];
  const u16* wmod = (const u16*)d_in[8];
  char* ws = (char*)d_ws;
  u16* modb = (u16*)(ws + WS_MODB);
  float2* st1 = (float2*)(ws + WS_ST1);
  float* kmax = (float*)(ws + WS_KMAX);
  float* uvp = (float*)(ws + WS_UVP);
  float2* st2 = (float2*)(ws + WS_ST2);
  u16* qf = (u16*)(ws + WS_Q);
  u16* kf = (u16*)(ws + WS_K);
  u16* vf = (u16*)(ws + WS_V);
  u16* of = (u16*)(ws + WS_O);
  u16* x1f = (u16*)(ws + WS_X1);
  u16* yf = (u16*)(ws + WS_Y);
  u16* hf = (u16*)(ws + WS_HH);
  k0_mod<<<224, 512, 0, stream>>>(c, wmod, x, modb, st1);
  k1_qkv<<<256, 256, 0, stream>>>(x, wqkv, wg, wu, modb, st1, qf, kf, vf, kmax, uvp);
  k2_attn<<<256, 512, 0, stream>>>(qf, kf, vf, kmax, of);
  k3_oproj<<<256, 256, 0, stream>>>(of, wo, x, modb, x1f, yf, st2);
  k4_gateup<<<240, 256, 0, stream>>>(yf, wg, wu, uvp, st2, hf);
  k5_down<<<256, 256, 0, stream>>>(hf, wd, x1f, modb, (u16*)d_out);
}
```

```cpp
#include <hip/hip_runtime.h>
#include <hip/hip_bf16.h>
#include <stdint.h>
#pragma clang diagnostic ignored "-Winline-asm"

typedef __attribute__((ext_vector_type(8))) __bf16 bf16x8;
typedef __attribute__((ext_vector_type(4))) short s16x4;
typedef __attribute__((ext_vector_type(8))) short s16x8;
typedef __attribute__((ext_vector_type(16))) float f32x16;
typedef __attribute__((ext_vector_type(4))) float f32x4;
typedef __attribute__((ext_vector_type(2))) float f32x2;
typedef __attribute__((ext_vector_type(4))) unsigned u32x4;
typedef __attribute__((ext_vector_type(2))) unsigned u32x2;
typedef unsigned short u16;
#define LDS_AS __attribute__((address_space(3)))
#define DI __device__ __forceinline__

constexpr int S_ = 2048, D_ = 512, H_ = 32, F_ = 1376;
constexpr int FB_ = F_ / 32;
constexpr int FK_ = F_ / 16;

constexpr size_t WS_MODB = 0;
constexpr size_t WS_ST1 = 8192;
constexpr size_t WS_KMAX = WS_ST1 + 16384;
constexpr size_t WS_UVP = WS_KMAX + 8192;
constexpr size_t WS_ST2 = WS_UVP + 45056;
constexpr size_t WS_Q = 1 << 20;
constexpr size_t WS_K = WS_Q + (2 << 20);
constexpr size_t WS_V = WS_K + (2 << 20);
constexpr size_t WS_O = WS_V + (2 << 20);
constexpr size_t WS_X1 = WS_O + (2 << 20);
constexpr size_t WS_Y = WS_X1 + (2 << 20);
constexpr size_t WS_HH = WS_Y + (2 << 20);

DI float asf(unsigned u) { return __builtin_bit_cast(float, u); }
DI float bf_lo(unsigned u) { return __builtin_bit_cast(float, u << 16); }
DI float bf_hi(unsigned u) { return __builtin_bit_cast(float, u & 0xffff0000u); }
DI float bf1(u16 u) { return __builtin_bit_cast(float, (unsigned)u << 16); }
typedef __attribute__((ext_vector_type(2))) __bf16 bf16x2;
DI unsigned pk2(float lo, float hi) {
  return __builtin_bit_cast(unsigned, __builtin_convertvector(f32x2{lo, hi}, bf16x2));
}
DI float bf_round(float x) { return bf_lo(pk2(x, x)); }

#define STAMP(k, i)
#define WAIT_V(n) asm volatile("s_waitcnt vmcnt(%0)" ::"n"(n) : "memory")
#define WAIT_L0() asm volatile("s_waitcnt lgkmcnt(0)" ::: "memory")
#define BAR() asm volatile("s_barrier" ::: "memory")

DI void glds16(const void* g, char* lds) {
  const unsigned a = (unsigned)(uintptr_t)(LDS_AS char*)lds;
  asm volatile("s_mov_b32 m0, %1\n\ts_nop 0\n\tglobal_load_lds_dwordx4 %0, off" ::"v"(g), "s"(a) : "m0", "memory");
}
DI void glds16s(const void* sbase, unsigned voff, char* lds) {
  const unsigned a = (unsigned)(uintptr_t)(LDS_AS char*)lds;
  asm volatile("s_mov_b32 m0, %2\n\ts_nop 0\n\tglobal_load_lds_dwordx4 %0, %1" ::"v"(voff), "s"(sbase), "s"(a)
               : "m0", "memory");
}
DI u32x4 aload16(const void* p) {
  u32x4 r;
  asm volatile("global_load_dwordx4 %0, %1, off" : "=v"(r) : "v"(p) : "memory");
  return r;
}
DI u32x2 aload8(const void* p) {
  u32x2 r;
  asm volatile("global_load_dwordx2 %0, %1, off" : "=v"(r) : "v"(p) : "memory");
  return r;
}
DI float aload4(const void* p) {
  float r;
  asm volatile("global_load_dword %0, %1, off" : "=v"(r) : "v"(p) : "memory");
  return r;
}
template <class T> DI void pin(T& v) { asm volatile("" : "+v"(v)); }

DI bf16x8 lds_frag(const char* p) { return *(const bf16x8*)p; }
typedef __attribute__((ext_vector_type(4))) short v4i16_t;
DI bf16x8 tr_frag(const char* p0, const char* p1) {
  v4i16_t a = __builtin_amdgcn_ds_read_tr16_b64_v4i16((LDS_AS v4i16_t*)p0);
  v4i16_t b = __builtin_amdgcn_ds_read_tr16_b64_v4i16((LDS_AS v4i16_t*)p1);
  s16x8 r = __builtin_shufflevector(a, b, 0, 1, 2, 3, 4, 5, 6, 7);
  return __builtin_bit_cast(bf16x8, r);
}
DI f32x16 mfma(bf16x8 a, bf16x8 b, f32x16 c) { return __builtin_amdgcn_mfma_f32_32x32x16_bf16(a, b, c, 0, 0, 0); }

template <int CTRL, int RM> DI float dppf(float v) {
  return __builtin_bit_cast(float, __builtin_amdgcn_update_dpp(0, __builtin_bit_cast(int, v), CTRL, RM, 0xf, true));
}
DI float wave_sum(float v) {
  v += dppf<0x111, 0xf>(v);
  v += dppf<0x112, 0xf>(v);
  v += dppf<0x114, 0xf>(v);
  v += dppf<0x118, 0xf>(v);
  v += dppf<0x142, 0xa>(v);
  v += dppf<0x143, 0xc>(v);
  return __builtin_bit_cast(float, __builtin_amdgcn_readlane(__builtin_bit_cast(int, v), 63));
}
DI float wave_max_nonneg(float v) {
  v = fmaxf(v, dppf<0x111, 0xf>(v));
  v = fmaxf(v, dppf<0x112, 0xf>(v));
  v = fmaxf(v, dppf<0x114, 0xf>(v));
  v = fmaxf(v, dppf<0x118, 0xf>(v));
  v = fmaxf(v, dppf<0x142, 0xa>(v));
  v = fmaxf(v, dppf<0x143, 0xc>(v));
  return __builtin_bit_cast(float, __builtin_amdgcn_readlane(__builtin_bit_cast(int, v), 63));
}
DI float other_half(float v) {
  unsigned u = __builtin_bit_cast(unsigned, v);
  auto r = __builtin_amdgcn_permlane32_swap(u, u, false, false);
  unsigned lo = r[0], hi = r[1];
  return __builtin_bit_cast(float, (__lane_id() < 32) ? hi : lo);
}

__global__ __launch_bounds__(512) void k0_mod(const u16* __restrict__ c, const u16* __restrict__ wmod,
                                              const u16* __restrict__ x, u16* __restrict__ modb,
                                              float2* __restrict__ st1) {
  __shared__ float red[128 * 36];
  __shared__ float red2[16 * 32];
  const int b = blockIdx.x, t = threadIdx.x;
  STAMP(0, 0)
  if (b < 96) {
    const int ch = t & 3, r = t >> 2;
    const u16* wp = wmod + (size_t)r * 3072 + b * 32 + ch * 8;
    u32x4 w[4];
#pragma unroll
    for (int i = 0; i < 4; i++) w[i] = *(const u32x4*)(wp + (size_t)i * 128 * 3072);
    float acc[8] = {0, 0, 0, 0, 0, 0, 0, 0};
#pragma unroll
    for (int i = 0; i < 4; i++) {
      float cv = bf1(c[r + 128 * i]);
      float s = bf_round(cv / (1.0f + __expf(-cv)));
#pragma unroll
      for (int j = 0; j < 4; j++) {
        acc[2 * j] += s * bf_lo(w[i][j]);
        acc[2 * j + 1] += s * bf_hi(w[i][j]);
      }
    }
    *(f32x4*)&red[r * 36 + ch * 8] = f32x4{acc[0], acc[1], acc[2], acc[3]};
    *(f32x4*)&red[r * 36 + ch * 8 + 4] = f32x4{acc[4], acc[5], acc[6], acc[7]};
    __syncthreads();
    {
      const int col = t & 31, part = t >> 5;
      float s = 0;
#pragma unroll
      for (int i = 0; i < 8; i++) s += red[(part * 8 + i) * 36 + col];
      red2[part * 32 + col] = s;
    }
    __syncthreads();
    if (t < 32) {
      float s = 0;
#pragma unroll
      for (int i = 0; i < 16; i++) s += red2[i * 32 + t];
      const int col = b * 32 + t, chunk = col >> 9;
      s = bf_round(s);
      if (chunk == 1 || chunk == 4) s = 1.0f + s;
      modb[col] = (u16)(pk2(s, s) & 0xffffu);
    }
  } else {
    const int w = t >> 6, lane = t & 63;
    const int row0 = (b - 96) * 16 + w * 2;
    u32x4 v[2];
#pragma unroll
    for (int i = 0; i < 2; i++) v[i] = *(const u32x4*)(x + (size_t)(row0 + i) * D_ + lane * 8);
#pragma unroll
    for (int i = 0; i < 2; i++) {
      float e[8];
#pragma unroll
      for (int j = 0; j < 4; j++) { e[2 * j] = bf_lo(v[i][j]); e[2 * j + 1] = bf_hi(v[i][j]); }
      float s = 0;
#pragma unroll
      for (int j = 0; j < 8; j++) s += e[j];
      const float mean = wave_sum(s) * (1.0f / D_);
      float q = 0;
#pragma unroll
      for (int j = 0; j < 8; j++) { float d = e[j] - mean; q += d * d; }
      const float var = wave_sum(q) * (1.0f / D_);
      const float rstd = 1.0f / sqrtf(var + 1e-5f);
      if (lane == 0) st1[row0 + i] = make_float2(rstd, -mean * rstd);
    }
  }
  STAMP(0, 1)
}

__global__ __launch_bounds__(256, 1) void k1_qkv(const u16* __restrict__ x, const u16* __restrict__ wqkv,
                                                 const u16* __restrict__ wg, const u16* __restrict__ wu,
                                                 const u16* __restrict__ modb, const float2* __restrict__ st1,
                                                 u16* __restrict__ qf, u16* __restrict__ kf, u16* __restrict__ vf,
                                                 float* __restrict__ kmax, float* __restrict__ uvp) {
  __shared__ __attribute__((aligned(1024))) char smem[163840];
  constexpr int RING = 65536, SLOT = 24576;
  const int t = threadIdx.x, lane = t & 63;
  const int w = __builtin_amdgcn_readfirstlane(t >> 6);
  const int xcd = blockIdx.x & 7, loc = blockIdx.x >> 3;
  const int rb = (xcd >> 1) * 8 + (loc & 7), cg = (xcd & 1) * 4 + (loc >> 3);
  const int row0 = rb * 64;

  STAMP(1, 0)
  u32x4 mB = aload16(modb + lane * 8);
  u32x4 mA = aload16(modb + 512 + lane * 8);
#pragma unroll
  for (int i = 0; i < 16; i++) {
    const int r = w * 16 + i;
    glds16(x + (size_t)(row0 + r) * D_ + ((lane ^ i) << 3), smem + r * 1024);
  }
  const int krow = (lane & 31) >> 2, nch = lane & 3, hb = lane >> 5;
  const u16* wsrc_qk = wqkv + (size_t)(w * 16 + krow) * 1536 + cg * 128 + hb * 32 + nch * 8;
  const u16* wsrc_v = wqkv + (size_t)(w * 16 + krow) * 1536 + 1024 + cg * 64 + hb * 32 + nch * 8;
#pragma unroll
  for (int c = 0; c < 4; c++) {
    char* dst = smem + RING + c * SLOT + w * 6144;
#pragma unroll
    for (int i = 0; i < 6; i++) {
      const size_t ro = (size_t)(c * 64 + (i / 3) * 8) * 1536;
      const u16* src = ((i % 3) == 2) ? wsrc_v + ro : wsrc_qk + ro + (i % 3) * 64;
      glds16(src, dst + i * 1024);
    }
  }
  STAMP(1, 1)
  WAIT_V(24);
  STAMP(1, 2)
  pin(mA); pin(mB);
  {
    float A[8], B[8];
#pragma unroll
    for (int j = 0; j < 4; j++) {
      A[2 * j] = bf_lo(mA[j]); A[2 * j + 1] = bf_hi(mA[j]);
      B[2 * j] = bf_lo(mB[j]); B[2 * j + 1] = bf_hi(mB[j]);
    }
#pragma unroll
    for (int i = 0; i < 16; i++) {
      const int r = w * 16 + i;
      const float2 st = st1[row0 + r];
      char* p = smem + r * 1024 + ((lane ^ i) << 4);
      u32x4 v = *(const u32x4*)p;
      u32x4 o;
#pragma unroll
      for (int j = 0; j < 4; j++) {
        float lo = (bf_lo(v[j]) * st.x + st.y) * A[2 * j] + B[2 * j];
        float hi = (bf_hi(v[j]) * st.x + st.y) * A[2 * j + 1] + B[2 * j + 1];
        o[j] = pk2(lo, hi);
      }
      *(u32x4*)p = o;
    }
  }
  f32x16 acc[3];
#pragma unroll
  for (int n = 0; n < 3; n++)
#pragma unroll
    for (int r = 0; r < 16; r++) acc[n][r] = 0.f;
  STAMP(1, 3)
  const int th = w & 1, role = w >> 1;
  const int hh = lane >> 5, r31 = lane & 31, r15 = lane & 15;
  {
    int aoff[8];
#pragma unroll
    for (int b = 0; b < 8; b++) aoff[b] = (th * 32 + r31) * 1024 + (((2 * b + hh) ^ r15) << 4);
    const int woff = hh * 3072 + (r15 >> 2) * 64 + ((lane >> 4) & 1) * 32 + (lane & 3) * 8 + role * 3 * 512;
#define K1_LOOP(ROLE)                                                                                       \
  _Pragma("unroll") for (int c = 0; c < 8; c++) {                                                           \
    if (c == 0) WAIT_V(18); else if (c <= 5) WAIT_V(12); else if (c == 6) WAIT_V(6); else WAIT_V(0);        \
    WAIT_L0();                                                                                              \
    BAR();                                                                                                  \
    if (c >= 1 && c + 3 < 8) {                                                                              \
      char* dst = smem + RING + ((c + 3) & 3) * SLOT + w * 6144;                                            \
      _Pragma("unroll") for (int i = 0; i < 6; i++) {                                                       \
        const size_t ro = (size_t)((c + 3) * 64 + (i / 3) * 8) * 1536;                                      \
        const u16* src = ((i % 3) == 2) ? wsrc_v + ro : wsrc_qk + ro + (i % 3) * 64;                        \
        glds16(src, dst + i * 1024);                                                                        \
      }                                                                                                     \
    }                                                                                                       \
    const char* ws = smem + RING + (c & 3) * SLOT + woff;                                                   \
    _Pragma("unroll") for (int s = 0; s < 4; s++) {                                                         \
      const int ks = c * 4 + s;                                                                             \
      bf16x8 a = lds_frag(smem + aoff[ks & 7] + (ks >> 3) * 256);                                           \
      _Pragma("unroll") for (int n = 0; n < 3; n++) {                                                       \
        bf16x8 wf = tr_frag(ws + s * 6144 + n * 512, ws + s * 6144 + n * 512 + 256);                        \
        if (ROLE == 1 && n >= 1) acc[n] = mfma(a, wf, acc[n]);                                              \
        else acc[n] = mfma(wf, a, acc[n]);                                                                  \
      }                                                                                                     \
    }                                                                                                       \
  }
    if (role == 0) { K1_LOOP(0) } else { K1_LOOP(1) }
#undef K1_LOOP
  }
  STAMP(1, 4)
  const int tb = rb * 2 + th;
#pragma unroll
  for (int n = 0; n < 3; n++) {
    const int nb = role * 3 + n;
    if (nb < 4) {
      const int blk = cg * 4 + nb;
      const bool isq = blk < 16;
      const float sc = isq ? 0.25f * 1.4426950408889634f : 1.0f;
      u16* dst = isq ? qf : kf;
#pragma unroll
      for (int e = 0; e < 2; e++) {
        const int head = (blk & 15) * 2 + e;
        u32x4 o;
        float n2 = 0;
#pragma unroll
        for (int j = 0; j < 4; j++) {
          float lo = acc[n][8 * e + 2 * j] * sc, hi = acc[n][8 * e + 2 * j + 1] * sc;
          n2 += lo * lo + hi * hi;
          o[j] = pk2(lo, hi);
        }
        *(u32x4*)(dst + ((size_t)(head * 64 + tb) * 64 + lane) * 8) = o;
        if (!isq) {
          n2 += other_half(n2);
          n2 = wave_max_nonneg(n2);
          if (lane == 0) kmax[head * 64 + tb] = n2;
        }
      }
    } else {
      const int head = (cg * 2 + (nb - 4)) * 2 + ((lane >> 4) & 1);
#pragma unroll
      for (int e = 0; e < 2; e++) {
        u32x4 o;
#pragma unroll
        for (int j = 0; j < 4; j++) o[j] = pk2(acc[n][8 * e + 2 * j], acc[n][8 * e + 2 * j + 1]);
        *(u32x4*)(vf + ((size_t)(head * 128 + tb * 2 + e) * 32 + hh * 16 + r15) * 8) = o;
      }
    }
  }
  STAMP(1, 5)
  if (blockIdx.x < 172) {
    const int g = blockIdx.x % 86, kh = blockIdx.x / 86;
    const u16* wm = (g < FB_) ? wg + g * 32 : wu + (g - FB_) * 32;
    const int ch = t & 3, r = t >> 2;
    u32x4 wv[4];
    float a2[4], sb[4];
#pragma unroll
    for (int i = 0; i < 4; i++) {
      const int k = kh * 256 + r + 64 * i;
      wv[i] = *(const u32x4*)(wm + (size_t)k * F_ + ch * 8);
      sb[i] = bf1(modb[1536 + k]);
      a2[i] = bf1(modb[2048 + k]);
    }
    float au[8] = {0, 0, 0, 0, 0, 0, 0, 0}, at[8] = {0, 0, 0, 0, 0, 0, 0, 0};
#pragma unroll
    for (int i = 0; i < 4; i++)
#pragma unroll
      for (int j = 0; j < 4; j++) {
        const float lo = bf_lo(wv[i][j]), hi = bf_hi(wv[i][j]);
        au[2 * j] += a2[i] * lo; au[2 * j + 1] += a2[i] * hi;
        at[2 * j] += sb[i] * lo; at[2 * j + 1] += sb[i] * hi;
      }
    __syncthreads();
    float* red = (float*)smem;
    *(f32x4*)&red[r * 68 + ch * 8] = f32x4{au[0], au[1], au[2], au[3]};
    *(f32x4*)&red[r * 68 + ch * 8 + 4] = f32x4{au[4], au[5], au[6], au[7]};
    *(f32x4*)&red[r * 68 + 32 + ch * 8] = f32x4{at[0], at[1], at[2], at[3]};
    *(f32x4*)&red[r * 68 + 32 + ch * 8 + 4] = f32x4{at[4], at[5], at[6], at[7]};
    __syncthreads();
    float* red2 = red + 64 * 68;
    {
      const int col = t & 63, part = t >> 6;
      float s = 0;
#pragma unroll
      for (int i = 0; i < 16; i++) s += red[(part * 16 + i) * 68 + col];
      red2[part * 64 + col] = s;
    }
    __syncthreads();
    if (t < 64) {
      const float s = red2[t] + red2[64 + t] + red2[128 + t] + red2[192 + t];
      uvp[(kh * 2 + (t >> 5)) * 2752 + g * 32 + (t & 31)] = s;
    }
  }
  STAMP(1, 6)
}

constexpr int A_K = 0, A_V = 65536, A_ONES = 131072;
DI void attn_tiles(const char* smem, int kaddr, int vaddr, int vstep, int t0, int t1, const bf16x8 qf, const f32x16& negm,
                   f32x16& o) {
  kaddr += t0 * 1024;
  vaddr += (t0 >> 2) * vstep;
  for (int t = t0; t < t1; t += 4) {
#pragma unroll
    for (int u = 0; u < 4; u++) {
      bf16x8 kfr = lds_frag(smem + kaddr + u * 1024);
      bf16x8 v0 = lds_frag(smem + vaddr + u * 1024);
      bf16x8 v1 = lds_frag(smem + vaddr + u * 1024 + 512);
      f32x16 s = mfma(kfr, qf, negm);
      u32x4 p0, p1;
#pragma unroll
      for (int j = 0; j < 4; j++) {
        p0[j] = pk2(__builtin_amdgcn_exp2f(s[2 * j]), __builtin_amdgcn_exp2f(s[2 * j + 1]));
        p1[j] = pk2(__builtin_amdgcn_exp2f(s[8 + 2 * j]), __builtin_amdgcn_exp2f(s[8 + 2 * j + 1]));
      }
      o = mfma(v0, __builtin_bit_cast(bf16x8, p0), o);
      o = mfma(v1, __builtin_bit_cast(bf16x8, p1), o);
    }
    kaddr += 4096;
    vaddr += vstep;
  }
}

__global__ __launch_bounds__(512, 2) void k2_attn(const u16* __restrict__ qf, const u16* __restrict__ kf,
                                                  const u16* __restrict__ vf, const float* __restrict__ kmax,
                                                  u16* __restrict__ of) {
  __shared__ __attribute__((aligned(1024))) char smem[131072 + 4096];
  const int t = threadIdx.x, lane = t & 63;
  const int w = __builtin_amdgcn_readfirstlane(t >> 6);
  const int head = blockIdx.x & 31, qg = blockIdx.x >> 5;
  const int tb = qg * 8 + w;
  STAMP(2, 0)
  u32x4 qraw = aload16(qf + ((size_t)(head * 64 + tb) * 64 + lane) * 8);
  float km = aload4(kmax + head * 64 + lane);
  const char* kg = (const char*)(kf + (size_t)head * 32768) + lane * 16;
  const char* vg = (const char*)(vf + (size_t)head * 32768) + lane * 16;
#pragma unroll
  for (int c = 0; c < 8; c++) {
    glds16(kg + (c * 8 + w) * 1024, smem + A_K + (c * 8 + w) * 1024);
    glds16(vg + (c * 8 + w) * 1024, smem + A_V + (c * 8 + w) * 1024);
  }
  if (t < 256) { const unsigned ov = (t & 31) == 0 ? 0x3f803f80u : 0u; *(u32x4*)(smem + A_ONES + t * 16) = u32x4{ov, ov, ov, ov}; }
  WAIT_V(14);
  pin(qraw); pin(km);
  float q2 = 0;
#pragma unroll
  for (int j = 0; j < 4; j++) { float lo = bf_lo(qraw[j]), hi = bf_hi(qraw[j]); q2 += lo * lo + hi * hi; }
  q2 += other_half(q2);
  const float k2 = wave_max_nonneg(km);
  const float mb = sqrtf(q2 * k2) * 1.01f;
  const bf16x8 qfr = __builtin_bit_cast(bf16x8, qraw);
  f32x16 negm, o;
#pragma unroll
  for (int r = 0; r < 16; r++) { negm[r] = -mb; o[r] = 0.f; }
  const int kaddr = A_K + lane * 16;
  const bool isv = (lane & 16) == 0;
  const int vaddr = isv ? A_V + ((lane >> 5) * 16 + (lane & 15)) * 16 : ((lane & 15) == 0 ? A_ONES : A_ONES + 16);
  const int vstep = isv ? 4096 : 0;
  WAIT_L0();
  BAR();
  STAMP(2, 1)
  attn_tiles(smem, kaddr, vaddr, vstep, 0, 8, qfr, negm, o);
  STAMP(2, 2)
  WAIT_V(0);
  BAR();
  STAMP(2, 3)
  attn_tiles(smem, kaddr, vaddr, vstep, 8, 64, qfr, negm, o);
  STAMP(2, 4)
  { float l0 = o[8] + other_half(o[8]); o[8] = l0; }
  if (__builtin_amdgcn_ballot_w64(!(o[8] >= 5.4e-20f)) != 0) {
    float m = -INFINITY;
    f32x16 z;
#pragma unroll
    for (int r = 0; r < 16; r++) z[r] = 0.f;
    for (int tt = 0; tt < 64; tt++) {
      f32x16 s = mfma(lds_frag(smem + kaddr + tt * 1024), qfr, z);
#pragma unroll
      for (int r = 0; r < 16; r++) m = fmaxf(m, s[r]);
    }
    m = fmaxf(m, other_half(m));
#pragma unroll
    for (int r = 0; r < 16; r++) { negm[r] = -m; o[r] = 0.f; }
    attn_tiles(smem, kaddr, vaddr, vstep, 0, 64, qfr, negm, o);
    { float l0 = o[8] + other_half(o[8]); o[8] = l0; }
  }
  const float inv = 1.0f / o[8];
  u32x4 r;
#pragma unroll
  for (int j = 0; j < 4; j++) r[j] = pk2(o[2 * j] * inv, o[2 * j + 1] * inv);
  *(u32x4*)(of + ((size_t)(tb * 32 + head) * 64 + lane) * 8) = r;
  STAMP(2, 5)
}

__global__ __launch_bounds__(256, 1) void k3_oproj(const u16* __restrict__ of, const u16* __restrict__ wo,
                                                   const u16* __restrict__ x, const u16* __restrict__ modb,
                                                   u16* __restrict__ x1f, u16* __restrict__ yf,
                                                   float2* __restrict__ st2) {
  __shared__ __attribute__((aligned(1024))) char smem[131072];
  const int t = threadIdx.x, lane = t & 63;
  const int w = __builtin_amdgcn_readfirstlane(t >> 6);
  const int xcd = blockIdx.x & 7, loc = blockIdx.x >> 3;
  const int rb = xcd * 4 + (loc & 3), cg = loc >> 2;
  const int th = w & 1, nh = w >> 1, hh = lane >> 5, r31 = lane & 31, r15 = lane & 15;
  const int tb = rb * 2 + th, n0 = cg * 64 + nh * 32;
  STAMP(3, 0)
  u32x2 xv[4], gv[4], av[4];
#pragma unroll
  for (int g = 0; g < 4; g++) {
    const int col = n0 + g * 8 + hh * 4;
    xv[g] = aload8(x + (size_t)(tb * 32 + r31) * D_ + col);
    gv[g] = aload8(modb + 1024 + col);
    av[g] = aload8(modb + 2048 + col);
  }
  const u16* asrc = of + ((size_t)(rb * 2 + (w >> 1)) * 32 + (w & 1) * 2) * 512 + lane * 8;
  const u16* wsrc = wo + (size_t)(w * 16 + ((lane & 31) >> 2)) * D_ + cg * 64 + (lane >> 5) * 32 + (lane & 3) * 8;
#pragma unroll
  for (int c = 0; c < 8; c++) {
    char* dst = smem + c * 16384;
    glds16(asrc + c * 4 * 512, dst + w * 2048);
    glds16(asrc + c * 4 * 512 + 512, dst + w * 2048 + 1024);
    glds16(wsrc + (size_t)c * 64 * D_, dst + 8192 + w * 2048);
    glds16(wsrc + (size_t)(c * 64 + 8) * D_, dst + 8192 + w * 2048 + 1024);
  }
  f32x16 acc[2];
#pragma unroll
  for (int r = 0; r < 16; r++) acc[0][r] = acc[1][r] = 0.f;
  const int aoff = th * 4096 + lane * 16;
  const int woff = 8192 + nh * 512 + (hh * 4 + (r15 >> 2)) * 64 + ((lane >> 4) & 1) * 32 + (lane & 3) * 8;
#pragma unroll
  for (int c = 0; c < 8; c++) {
    WAIT_V(4 * (7 - c));
    BAR();
    if (c == 0) { STAMP(3, 1) }
    if (c == 7) { STAMP(3, 2) }
    const char* base = smem + c * 16384;
#pragma unroll
    for (int s = 0; s < 4; s++) {
      bf16x8 a = lds_frag(base + aoff + s * 1024);
      bf16x8 wf = tr_frag(base + woff + s * 2048, base + woff + s * 2048 + 1024);
      acc[s & 1] = mfma(wf, a, acc[s & 1]);
    }
  }
#pragma unroll
  for (int g = 0; g < 4; g++) { pin(xv[g]); pin(gv[g]); pin(av[g]); }
  STAMP(3, 3)
  float x1[16];
  float sum = 0;
#pragma unroll
  for (int g = 0; g < 4; g++)
#pragma unroll
    for (int j = 0; j < 2; j++) {
      const int r = g * 4 + j * 2;
      float lo = bf_lo(xv[g][j]) + bf_lo(gv[g][j]) * (acc[0][r] + acc[1][r]);
      float hi = bf_hi(xv[g][j]) + bf_hi(gv[g][j]) * (acc[0][r + 1] + acc[1][r + 1]);
      const unsigned p = pk2(lo, hi);
      xv[g][j] = p;
      x1[r] = bf_lo(p); x1[r + 1] = bf_hi(p);
      sum += x1[r] + x1[r + 1];
    }
  sum += other_half(sum);
  const float mean = sum * (1.0f / 32.0f);
  float m2 = 0;
#pragma unroll
  for (int r = 0; r < 16; r++) { const float d = x1[r] - mean; m2 += d * d; }
  m2 += other_half(m2);
  if (lane < 32) st2[(size_t)(tb * 32 + r31) * 16 + cg * 2 + nh] = make_float2(mean, m2);
#pragma unroll
  for (int e = 0; e < 2; e++) {
    const size_t o = ((size_t)(tb * 32 + cg * 4 + nh * 2 + e) * 64 + lane) * 8;
    *(u32x4*)(x1f + o) = u32x4{xv[2 * e][0], xv[2 * e][1], xv[2 * e + 1][0], xv[2 * e + 1][1]};
    u32x4 y;
#pragma unroll
    for (int j = 0; j < 4; j++) {
      const int g = 2 * e + (j >> 1), jj = j & 1, r = g * 4 + jj * 2;
      y[j] = pk2(x1[r] * bf_lo(av[g][jj]), x1[r + 1] * bf_hi(av[g][jj]));
    }
    *(u32x4*)(yf + o) = y;
  }
  STAMP(3, 4)
}

__global__ __launch_bounds__(256, 1) void k4_gateup(const u16* __restrict__ yf, const u16* __restrict__ wg,
                                                    const u16* __restrict__ wu, const float* __restrict__ uvp,
                                                    const float2* __restrict__ st2, u16* __restrict__ hf) {
  constexpr int SLOT = 20480, R = 7, XTRA = SLOT * R;
  __shared__ __attribute__((aligned(1024))) char smem[XTRA + 2048];
  const int t = threadIdx.x, lane = t & 63;
  const int w = __builtin_amdgcn_readfirstlane(t >> 6);
  const int xcd = blockIdx.x & 7, loc = blockIdx.x >> 3;
  const int rb = xcd * 2 + (loc & 1), cg = loc >> 1;
  const int hh = lane >> 5, r31 = lane & 31, r15 = lane & 15;
  const int tb = rb * 4 + w;
  STAMP(4, 0)
  u32x4 sraw[4];
#pragma unroll
  for (int i = 0; i < 4; i++) sraw[i] = aload16((const char*)(st2 + (size_t)(tb * 32 + r31) * 16 + hh * 8) + i * 16);
  float uv0 = 0, uv1 = 0, uv2 = 0, uv3 = 0;
  {
    const int tt = t < 192 ? t : 0;
    const int nb = tt >> 5;
    const int fb = min(cg * 3 + (nb % 3), FB_ - 1);
    const int col = (nb / 3) * F_ + fb * 32 + (tt & 31);
    uv0 = aload4(uvp + col); uv1 = aload4(uvp + 2752 + col); uv2 = aload4(uvp + 2 * 2752 + col); uv3 = aload4(uvp + 3 * 2752 + col);
  }
  const u16* asrc = yf + (size_t)tb * 32 * 512 + lane * 8;
  const u16* wsrc[3];
#pragma unroll
  for (int i = 0; i < 3; i++) {
    const int nb = 2 * i + hh;
    const int fb = min(cg * 3 + (nb % 3), FB_ - 1);
    wsrc[i] = (nb < 3 ? wg : wu) + (size_t)(w * 8 + (r31 >> 2)) * F_ + fb * 32 + (lane & 3) * 8;
  }
#define K4_ISSUE(c)                                                                     \
  {                                                                                     \
    char* dst = smem + ((c) % R) * SLOT;                                                \
    glds16(asrc + (c) * 1024, dst + w * 2048);                                          \
    glds16(asrc + (c) * 1024 + 512, dst + w * 2048 + 1024);                             \
    _Pragma("unroll") for (int i = 0; i < 3; i++)                                       \
        glds16(wsrc[i] + (size_t)(c) * 32 * F_, dst + 8192 + w * 3072 + i * 1024);      \
  }
#pragma unroll
  for (int c = 0; c < R; c++) K4_ISSUE(c)
  f32x16 acc[6];
#pragma unroll
  for (int n = 0; n < 6; n++)
#pragma unroll
    for (int r = 0; r < 16; r++) acc[n][r] = 0.f;
  const int aoff = w * 2048 + lane * 16;
  const int woff = 8192 + (hh * 4 + (r15 >> 2)) * 64 + ((lane >> 4) & 1) * 32 + (lane & 3) * 8;
#pragma unroll
  for (int c = 0; c < 16; c++) {
    const int hi = (c == 0) ? R - 1 : (c + R - 2 < 15 ? c + R - 2 : 15);
    WAIT_V(5 * (hi - c));
    if (c == 0) {
      pin(uv0); pin(uv1); pin(uv2); pin(uv3);
      if (t < 192) {
        float* xs = (float*)(smem + XTRA);
        xs[t] = uv0 + uv2;
        xs[192 + t] = uv1 + uv3;
      }
    }
    WAIT_L0();
    BAR();
    if (c == 0) { STAMP(4, 1) }
    if (c == 8) { STAMP(4, 2) }
    if (c == 15) { STAMP(4, 3) }
    if (c >= 1 && c + R - 1 < 16) K4_ISSUE(c + R - 1)
    const char* base = smem + (c % R) * SLOT;
#pragma unroll
    for (int s = 0; s < 2; s++) {
      bf16x8 a = lds_frag(base + aoff + s * 1024);
#pragma unroll
      for (int n = 0; n < 6; n++) {
        bf16x8 wf = tr_frag(base + woff + s * 6144 + n * 512, base + woff + s * 6144 + 3072 + n * 512);
        acc[n] = mfma(wf, a, acc[n]);
      }
    }
  }
#undef K4_ISSUE
  STAMP(4, 4)
#pragma unroll
  for (int i = 0; i < 4; i++) pin(sraw[i]);
  float msum = 0, m2 = 0;
#pragma unroll
  for (int i = 0; i < 4; i++) {
    msum += asf(sraw[i][0]) + asf(sraw[i][2]);
    m2 += asf(sraw[i][1]) + asf(sraw[i][3]);
  }
  msum += other_half(msum);
  const float mean = msum * (1.0f / 16.0f);
#pragma unroll
  for (int i = 0; i < 4; i++) {
    const float d0 = asf(sraw[i][0]) - mean, d1 = asf(sraw[i][2]) - mean;
    m2 += 32.0f * (d0 * d0 + d1 * d1);
  }
  m2 += other_half(m2);
  const float rstd = 1.0f / sqrtf(m2 * (1.0f / D_) + 1e-5f);
  const float nm = -mean * rstd;
  const float* xs = (const float*)(smem + XTRA);
#pragma unroll
  for (int n = 0; n < 3; n++) {
    const int fb = cg * 3 + n;
    if (fb < FB_) {
      unsigned pk[8];
#pragma unroll
      for (int g = 0; g < 4; g++) {
        const int cidx = n * 32 + g * 8 + hh * 4;
        const f32x4 ug = *(const f32x4*)(xs + cidx), tg = *(const f32x4*)(xs + 192 + cidx);
        const f32x4 uu = *(const f32x4*)(xs + 96 + cidx), tu = *(const f32x4*)(xs + 192 + 96 + cidx);
        float hv[4];
#pragma unroll
        for (int j = 0; j < 4; j++) {
          const float gt = acc[n][g * 4 + j] * rstd + (nm * ug[j] + tg[j]);
          const float up = acc[n + 3][g * 4 + j] * rstd + (nm * uu[j] + tu[j]);
          hv[j] = up * __builtin_amdgcn_rcpf(1.0f + __builtin_amdgcn_exp2f(-1.4426950408889634f * gt));
        }
        pk[2 * g] = pk2(hv[0], hv[1]);
        pk[2 * g + 1] = pk2(hv[2], hv[3]);
      }
#pragma unroll
      for (int e = 0; e < 2; e++)
        *(u32x4*)(hf + ((size_t)(tb * FK_ + fb * 2 + e) * 64 + lane) * 8) =
            u32x4{pk[4 * e], pk[4 * e + 1], pk[4 * e + 2], pk[4 * e + 3]};
    }
  }
  STAMP(4, 5)
}

__global__ __launch_bounds__(256, 1) void k5_down(const u16* __restrict__ hf, const u16* __restrict__ wd,
                                                  const u16* __restrict__ x1f, const u16* __restrict__ modb,
                                                  u16* __restrict__ out) {
  constexpr int SLOT = 32768, R = 5;
  __shared__ __attribute__((aligned(1024))) char smem[SLOT * R];
  const int t = threadIdx.x, lane = t & 63;
  const int w = __builtin_amdgcn_readfirstlane(t >> 6);
  const int xcd = blockIdx.x & 7, loc = blockIdx.x >> 3;
  const int rb = xcd * 4 + (loc & 3), cg = loc >> 2;
  const int th = w & 1, nh = w >> 1, hh = lane >> 5, r31 = lane & 31, r15 = lane & 15;
  const int tb = rb * 2 + th, n0 = cg * 64 + nh * 32;
  STAMP(5, 0)
  u32x4 xv[2];
  u32x2 gv[4];
#pragma unroll
  for (int e = 0; e < 2; e++) xv[e] = aload16(x1f + ((size_t)(tb * 32 + cg * 4 + nh * 2 + e) * 64 + lane) * 8);
#pragma unroll
  for (int g = 0; g < 4; g++) gv[g] = aload8(modb + 2560 + n0 + g * 8 + hh * 4);
  const u16* asrc = hf + (size_t)(rb * 2 + (w >> 1)) * FK_ * 512 + lane * 8;
  const u16* wsrc = wd + (size_t)(r31 >> 2) * D_ + cg * 64 + hh * 32 + (lane & 3) * 8;
#define K5_ISSUE(c)                                                                                    \
  {                                                                                                    \
    char* dst = smem + ((c) % R) * SLOT;                                                               \
    constexpr int NP = ((c) == 10) ? 3 : 4;                                                            \
    _Pragma("unroll") for (int i = 0; i < NP; i++) {                                                   \
      const int ks = (w & 1) * NP + i;                                                                 \
      glds16(asrc + ((c) * 8 + ks) * 512, dst + (w >> 1) * 8192 + ks * 1024);                          \
    }                                                                                                  \
    _Pragma("unroll") for (int i = 0; i < NP; i++) {                                                   \
      const int kb = w * NP + i;                                                                       \
      glds16(wsrc + (size_t)((c) * 128 + kb * 8) * D_, dst + 16384 + kb * 1024);                       \
    }                                                                                                  \
  }
  K5_ISSUE(0) K5_ISSUE(1) K5_ISSUE(2) K5_ISSUE(3) K5_ISSUE(4)
  f32x16 acc[2];
#pragma unroll
  for (int r = 0; r < 16; r++) acc[0][r] = acc[1][r] = 0.f;
  const int aoff = th * 8192 + lane * 16;
  const int woff = 16384 + nh * 512 + (hh * 4 + (r15 >> 2)) * 64 + ((lane >> 4) & 1) * 32 + (lane & 3) * 8;
#define K5_STEP(c, nwait, nissue)                                                                      \
  {                                                                                                    \
    WAIT_V(nwait);                                                                                     \
    WAIT_L0();                                                                                         \
    BAR();                                                                                             \
    if (nissue >= 0) K5_ISSUE((nissue >= 0 ? nissue : 0))                                              \
    const char* base = smem + ((c) % R) * SLOT;                                                        \
    _Pragma("unroll") for (int s = 0; s < ((c) == 10 ? 6 : 8); s++) {                                  \
      bf16x8 a = lds_frag(base + aoff + s * 1024);                                                     \
      bf16x8 wf = tr_frag(base + woff + s * 2048, base + woff + s * 2048 + 1024);                      \
      acc[s & 1] = mfma(wf, a, acc[s & 1]);                                                            \
    }                                                                                                  \
  }
  K5_STEP(0, 32, -1)
  STAMP(5, 1)
  K5_STEP(1, 24, 5)
  K5_STEP(2, 24, 6)
  K5_STEP(3, 24, 7)
  K5_STEP(4, 24, 8)
  K5_STEP(5, 24, 9)
  STAMP(5, 2)
  K5_STEP(6, 24, 10)
  K5_STEP(7, 22, -1)
  K5_STEP(8, 14, -1)
  K5_STEP(9, 6, -1)
  K5_STEP(10, 0, -1)
#undef K5_STEP
#undef K5_ISSUE
  STAMP(5, 3)
  pin(xv[0]); pin(xv[1]);
#pragma unroll
  for (int g = 0; g < 4; g++) pin(gv[g]);
  unsigned pk[8];
#pragma unroll
  for (int g = 0; g < 4; g++)
#pragma unroll
    for (int j = 0; j < 2; j++) {
      const int r = g * 4 + j * 2;
      const unsigned xr = xv[g >> 1][(g & 1) * 2 + j];
      const float lo = bf_lo(xr) + bf_lo(gv[g][j]) * (acc[0][r] + acc[1][r]);
      const float hi = bf_hi(xr) + bf_hi(gv[g][j]) * (acc[0][r + 1] + acc[1][r + 1]);
      pk[g * 2 + j] = pk2(lo, hi);
    }
  u16* orow = out + (size_t)(tb * 32 + r31) * D_ + n0;
#pragma unroll
  for (int e = 0; e < 2; e++) {
    auto r0 = __builtin_amdgcn_permlane32_swap(pk[4 * e], pk[4 * e + 2], false, false);
    auto r1 = __builtin_amdgcn_permlane32_swap(pk[4 * e + 1], pk[4 * e + 3], false, false);
    const u32x4 v = u32x4{r0[0], r1[0], r0[1], r1[1]};
    *(u32x4*)(orow + e * 16 + hh * 8) = v;
  }
  STAMP(5, 4)
}

extern "C" void kernel_launch(void* const* d_in, const int* in_sizes, int n_in, void* d_out, int out_size, void* d_ws,
                              size_t ws_size, hipStream_t stream) {
  const u16* x = (const u16*)d_in[0];
  const u16* wqkv = (const u16*)d_in[1];
  const u16* wo = (const u16*)d_in[2];
  const u16* wg = (const u16*)d_in[4];
  const u16* wu = (const u16*)d_in[5];
  const u16* wd = (const u16*)d_in[6];
  const u16* c = (const u16*)d_in[7];
  const u16* wmod = (const u16*)d_in[8];
  char* ws = (char*)d_ws;
  u16* modb = (u16*)(ws + WS_MODB);
  float2* st1 = (float2*)(ws + WS_ST1);
  float* kmax = (float*)(ws + WS_KMAX);
  float* uvp = (float*)(ws + WS_UVP);
  float2* st2 = (float2*)(ws + WS_ST2);
  u16* qf = (u16*)(ws + WS_Q);
  u16* kf = (u16*)(ws + WS_K);
  u16* vf = (u16*)(ws + WS_V);
  u16* of = (u16*)(ws + WS_O);
  u16* x1f = (u16*)(ws + WS_X1);
  u16* yf = (u16*)(ws + WS_Y);
  u16* hf = (u16*)(ws + WS_HH);
  k0_mod<<<224, 512, 0, stream>>>(c, wmod, x, modb, st1);
  k1_qkv<<<256, 256, 0, stream>>>(x, wqkv, wg, wu, modb, st1, qf, kf, vf, kmax, uvp);
  k2_attn<<<256, 512, 0, stream>>>(qf, kf, vf, kmax, of);
  k3_oproj<<<256, 256, 0, stream>>>(of, wo, x, modb, x1f, yf, st2);
  k4_gateup<<<240, 256, 0, stream>>>(yf, wg, wu, uvp, st2, hf);
  k5_down<<<256, 256, 0, stream>>>(hf, wd, x1f, modb, (u16*)d_out);
}
```

```cpp
#include <hip/hip_runtime.h>
#include <hip/hip_bf16.h>
#include <stdint.h>
#pragma clang diagnostic ignored "-Winline-asm"

typedef __attribute__((ext_vector_type(8))) __bf16 bf16x8;
typedef __attribute__((ext_vector_type(2))) __bf16 bf16x2;
typedef __attribute__((ext_vector_type(8))) short s16x8;
typedef __attribute__((ext_vector_type(4))) short v4i16_t;
typedef __attribute__((ext_vector_type(16))) float f32x16;
typedef __attribute__((ext_vector_type(4))) float f32x4;
typedef __attribute__((ext_vector_type(2))) float f32x2;
typedef __attribute__((ext_vector_type(4))) unsigned u32x4;
typedef __attribute__((ext_vector_type(2))) unsigned u32x2;
typedef unsigned short u16;
typedef unsigned long long u64;
#define LDS_AS __attribute__((address_space(3)))
#define DI __device__ __forceinline__

constexpr int D_ = 512, F_ = 1376, FK_ = F_ / 16;

constexpr size_t WS_EPOCH = 0;
constexpr size_t WS_FLAG = 4096;
constexpr size_t WS_MODA = WS_FLAG + 131072;
constexpr size_t WS_MODB = WS_MODA + 8192;
constexpr size_t WS_KMAX = WS_MODB + 16384;
constexpr size_t WS_ST2 = WS_KMAX + 8192;
constexpr size_t WS_Q = 1 << 20;
constexpr size_t WS_K = WS_Q + (2 << 20);
constexpr size_t WS_V = WS_K + (2 << 20);
constexpr size_t WS_O = WS_V + (2 << 20);
constexpr size_t WS_Y = WS_O + (2 << 20);
constexpr size_t WS_HH = WS_Y + (2 << 20);
constexpr size_t WS_WT = WS_HH + (6 << 20);

DI float asf(unsigned u) { return __builtin_bit_cast(float, u); }
DI unsigned asu(float f) { return __builtin_bit_cast(unsigned, f); }
DI float bf_lo(unsigned u) { return __builtin_bit_cast(float, u << 16); }
DI float bf_hi(unsigned u) { return __builtin_bit_cast(float, u & 0xffff0000u); }
DI unsigned pk2(float lo, float hi) {
  return __builtin_bit_cast(unsigned, __builtin_convertvector(f32x2{lo, hi}, bf16x2));
}
DI float bf_round(float x) { return bf_lo(pk2(x, x)); }
DI float dot2(unsigned a, unsigned b, float c) {
  return __builtin_amdgcn_fdot2_f32_bf16(__builtin_bit_cast(bf16x2, a), __builtin_bit_cast(bf16x2, b), c, false);
}

#define STAMP(k, i)
#define WAIT_V(n) asm volatile("s_waitcnt vmcnt(%0)" ::"n"(n) : "memory")
#define BAR() asm volatile("s_waitcnt lgkmcnt(0)\n\ts_barrier" ::: "memory")
#define SB() __builtin_amdgcn_sched_barrier(0)

DI void glds(const void* sbase, unsigned voff, unsigned lds) {
  asm volatile("s_mov_b32 m0, %2\n\ts_nop 0\n\tglobal_load_lds_dwordx4 %0, %1" ::"v"(voff), "s"(sbase), "s"(lds)
               : "m0", "memory");
}
DI void glds_c(const void* sbase, unsigned voff, unsigned lds) {
  asm volatile("s_mov_b32 m0, %2\n\ts_nop 0\n\tglobal_load_lds_dwordx4 %0, %1 sc1" ::"v"(voff), "s"(sbase), "s"(lds)
               : "m0", "memory");
}
DI u32x4 ld16(const void* sbase, unsigned voff) {
  u32x4 r;
  asm volatile("global_load_dwordx4 %0, %1, %2" : "=v"(r) : "v"(voff), "s"(sbase) : "memory");
  return r;
}
DI u32x4 ld16p(const void* p) {
  u32x4 r;
  asm volatile("global_load_dwordx4 %0, %1, off" : "=v"(r) : "v"(p) : "memory");
  return r;
}
DI u32x4 ld16c(const void* sbase, unsigned voff) {
  u32x4 r;
  asm volatile("global_load_dwordx4 %0, %1, %2 sc1" : "=v"(r) : "v"(voff), "s"(sbase) : "memory");
  return r;
}
DI u32x2 ld8(const void* sbase, unsigned voff) {
  u32x2 r;
  asm volatile("global_load_dwordx2 %0, %1, %2" : "=v"(r) : "v"(voff), "s"(sbase) : "memory");
  return r;
}
DI unsigned ld4c(const void* sbase, unsigned voff) {
  unsigned r;
  asm volatile("global_load_dword %0, %1, %2 sc1" : "=v"(r) : "v"(voff), "s"(sbase) : "memory");
  return r;
}
DI unsigned ld2(const void* sbase, unsigned voff) {
  unsigned r;
  asm volatile("global_load_ushort %0, %1, %2" : "=v"(r) : "v"(voff), "s"(sbase) : "memory");
  return r;
}
DI void st16c(void* sbase, unsigned voff, u32x4 v) {
  asm volatile("global_store_dwordx4 %0, %1, %2 sc1\n\ts_nop 1" ::"v"(voff), "v"(v), "s"(sbase) : "memory");
}
DI void st8c(void* sbase, unsigned voff, u32x2 v) {
  asm volatile("global_store_dwordx2 %0, %1, %2 sc1\n\ts_nop 1" ::"v"(voff), "v"(v), "s"(sbase) : "memory");
}
DI void st4c(void* sbase, unsigned voff, unsigned v) {
  asm volatile("global_store_dword %0, %1, %2 sc1\n\ts_nop 1" ::"v"(voff), "v"(v), "s"(sbase) : "memory");
}
template <class T> DI void pin(T& v) { asm volatile("" : "+v"(v)); }

DI bf16x8 lds_frag(const char* p) { return *(const bf16x8*)p; }
DI bf16x8 tr_frag(const char* p0, const char* p1) {
  v4i16_t a = __builtin_amdgcn_ds_read_tr16_b64_v4i16((LDS_AS v4i16_t*)p0);
  v4i16_t b = __builtin_amdgcn_ds_read_tr16_b64_v4i16((LDS_AS v4i16_t*)p1);
  s16x8 r = __builtin_shufflevector(a, b, 0, 1, 2, 3, 4, 5, 6, 7);
  return __builtin_bit_cast(bf16x8, r);
}
DI bf16x8 asfrag(u32x4 v) { return __builtin_bit_cast(bf16x8, v); }
DI f32x16 mfma(bf16x8 a, bf16x8 b, f32x16 c) { return __builtin_amdgcn_mfma_f32_32x32x16_bf16(a, b, c, 0, 0, 0); }

template <int CTRL, int RM> DI float dppf(float v) {
  return __builtin_bit_cast(float, __builtin_amdgcn_update_dpp(0, __builtin_bit_cast(int, v), CTRL, RM, 0xf, true));
}
DI float wave_max_nonneg(float v) {
  v = fmaxf(v, dppf<0x111, 0xf>(v));
  v = fmaxf(v, dppf<0x112, 0xf>(v));
  v = fmaxf(v, dppf<0x114, 0xf>(v));
  v = fmaxf(v, dppf<0x118, 0xf>(v));
  v = fmaxf(v, dppf<0x142, 0xa>(v));
  v = fmaxf(v, dppf<0x143, 0xc>(v));
  return __builtin_bit_cast(float, __builtin_amdgcn_readlane(__builtin_bit_cast(int, v), 63));
}
DI float sum32(float v) {
  const unsigned u = asu(v);
  auto r = __builtin_amdgcn_permlane32_swap(u, u, false, false);
  const unsigned a = r[0], b = r[1];
  return asf(a) + asf(b);
}
DI float max32(float v) {
  const unsigned u = asu(v);
  auto r = __builtin_amdgcn_permlane32_swap(u, u, false, false);
  const unsigned a = r[0], b = r[1];
  return fmaxf(asf(a), asf(b));
}
DI float sum16(float v) {
  const unsigned u = asu(v);
  auto r = __builtin_amdgcn_permlane16_swap(u, u, false, false);
  const unsigned a = r[0], b = r[1];
  return asf(a) + asf(b);
}
DI float sum_mod4(float v) {
  v += dppf<0x124, 0xf>(v);
  v += dppf<0x128, 0xf>(v);
  return sum32(sum16(v));
}
DI float wave_sum(float v) {
  v += dppf<0x121, 0xf>(v);
  v += dppf<0x122, 0xf>(v);
  return sum_mod4(v);
}

DI float sum_mod8(float v) {
  v += dppf<0x128, 0xf>(v);
  return sum32(sum16(v));
}
DI void mod_partial(const u32x4 (&wm)[8], u32x4 cw, int lane, float* red) {
  float acc[8] = {0, 0, 0, 0, 0, 0, 0, 0};
#pragma unroll
  for (int i = 0; i < 8; i++) {
    const float c = (i & 1) ? bf_hi(cw[i >> 1]) : bf_lo(cw[i >> 1]);
    const float s = bf_round(c * __builtin_amdgcn_rcpf(1.0f + __builtin_amdgcn_exp2f(-1.4426950408889634f * c)));
#pragma unroll
    for (int j = 0; j < 4; j++) {
      acc[2 * j] += s * bf_lo(wm[i][j]);
      acc[2 * j + 1] += s * bf_hi(wm[i][j]);
    }
  }
#pragma unroll
  for (int j = 0; j < 8; j++) acc[j] = sum_mod8(acc[j]);
  if (lane < 8) {
    *(f32x4*)(red + lane * 8) = f32x4{acc[0], acc[1], acc[2], acc[3]};
    *(f32x4*)(red + lane * 8 + 4) = f32x4{acc[4], acc[5], acc[6], acc[7]};
  }
}
DI void mod_publish(const float* red, int lane, bool plus1, char* dst, u64 T) {
  float s = 0;
#pragma unroll
  for (int i = 0; i < 8; i++) s += red[i * 64 + lane];
  s = bf_round(s);
  if (plus1) s += 1.0f;
  st8c(dst, lane * 8, u32x2{((unsigned)T << 16) | (pk2(s, s) & 0xffffu), (unsigned)(T >> 32)});
}

constexpr unsigned SPIN_CAP = 1u << 18;
DI void poll_flags(const void* sbase, unsigned voff, u64 tag) {
  for (unsigned s = 0; s < SPIN_CAP; s++) {
    u32x2 v;
    asm volatile("global_load_dwordx2 %0, %1, %2 sc1\n\ts_waitcnt vmcnt(0)" : "=v"(v) : "v"(voff), "s"(sbase) : "memory");
    const bool ok = v[0] == (unsigned)tag && v[1] == (unsigned)(tag >> 32);
    if (__builtin_amdgcn_ballot_w64(!ok) == 0) break;
    __builtin_amdgcn_s_sleep(1);
  }
}

constexpr int L1_W = 0, L1_X = 98304, L1_AB = 147456, L1_ST = 153600, L1_RED = 155648;
constexpr int L2_ONES = 131072, L2_MB = 147456, L2_UT = 151552, L2_FAIL = 153600, L3_RED = 65536;
constexpr int L4_UT = 159744, L4_ST = 161792;

DI void attn_slow(const char* smem, int kaddr, int vaddr, int vstep, int t1, const bf16x8 qf, const f32x16& negm,
                  f32x16& o) {
  for (int t = 0; t < t1; t += 4) {
#pragma unroll
    for (int u = 0; u < 4; u++) {
      bf16x8 kfr = lds_frag(smem + kaddr + u * 2048);
      bf16x8 v0 = lds_frag(smem + vaddr + u * 2048 + 1024);
      bf16x8 v1 = lds_frag(smem + vaddr + u * 2048 + 1536);
      f32x16 s = mfma(kfr, qf, negm);
      u32x4 p0, p1;
#pragma unroll
      for (int j = 0; j < 4; j++) {
        p0[j] = pk2(__builtin_amdgcn_exp2f(s[2 * j]), __builtin_amdgcn_exp2f(s[2 * j + 1]));
        p1[j] = pk2(__builtin_amdgcn_exp2f(s[8 + 2 * j]), __builtin_amdgcn_exp2f(s[8 + 2 * j + 1]));
      }
      o = mfma(v0, asfrag(p0), o);
      o = mfma(v1, asfrag(p1), o);
    }
    kaddr += 8192;
    vaddr += vstep;
  }
}

__global__ __launch_bounds__(512, 2) void fused_block(const u16* __restrict__ x, const u16* __restrict__ wqkv,
                                                      const u16* __restrict__ wo, const u16* __restrict__ wg,
                                                      const u16* __restrict__ wu, const u16* __restrict__ wd,
                                                      const u16* __restrict__ cvec, const u16* __restrict__ wmod,
                                                      u16* __restrict__ out, char* __restrict__ ws) {
  __shared__ __attribute__((aligned(1024))) char smem[163840];
  const unsigned lds0 = (unsigned)(uintptr_t)(LDS_AS char*)smem;
  const int w = __builtin_amdgcn_readfirstlane(threadIdx.x >> 6);
  const int b = blockIdx.x, g = b & 7, loc = b >> 3;
#define LANE_VARS                                                                   \
  int t = threadIdx.x;                                                              \
  asm volatile("" : "+v"(t));                                                       \
  const int lane = t & 63, hh = lane >> 5, r31 = lane & 31, r15 = lane & 15;        \
  const unsigned l16 = lane * 16;                                                   \
  const int prow = hh * 8 + (r31 >> 2), pch = lane & 3;                             \
  const int trc = ((lane >> 4) & 1) * 32 + (lane & 3) * 8;                          \
  const int woff_std = hh * 512 + (r15 >> 2) * 64 + trc;   \
  const int woff_fm = (hh * 4 + (r15 >> 2)) * 64 + trc;    \
  (void)l16; (void)prow; (void)pch; (void)woff_std; (void)woff_fm; (void)r31; (void)r15;
  char* const flags = ws + WS_FLAG;
  STAMP(0, 0)

  u64 T, epoch;
  {
    LANE_VARS
    const int rb = (g >> 1) * 4 + (loc & 3), cg = (g & 1) * 8 + (loc >> 2);
    const int tb4 = w & 3, kh = w >> 2;
    const unsigned wm_vo = (unsigned)((lane >> 3) * 8 * 3072 + (lane & 7) * 8) * 2;
    const u16* const wm_p = wmod + (size_t)w * 64 * 3072 + b * 64;
    const u16* xb = x + (size_t)(rb * 128 + tb4 * 32) * D_ + kh * 256;
    const unsigned x_vo = (unsigned)((lane >> 3) * D_ + (lane & 7) * 8) * 2;
    u32x4 xr[16];
    asm volatile("s_load_dwordx2 %0, %1, 0x0" : "=s"(epoch) : "s"(ws + WS_EPOCH) : "memory");
    if (b < 16) {
      u32x4 wm[8];
#pragma unroll
      for (int i = 0; i < 8; i++) wm[i] = ld16(wm_p + i * 3072, wm_vo);
      u32x4 cw = ld16(cvec + w * 64, (lane >> 3) * 16);
#pragma unroll
      for (int i = 0; i < 6; i++) xr[i] = ld16(xb + (i & 3) * 8 * D_ + (i >> 2) * 64, x_vo);
      WAIT_V(6);
#pragma unroll
      for (int i = 0; i < 8; i++) pin(wm[i]);
      pin(cw);
      mod_partial(wm, cw, lane, (float*)(smem + L1_RED) + w * 64);
    } else {
#pragma unroll
      for (int i = 0; i < 6; i++) xr[i] = ld16(xb + (i & 3) * 8 * D_ + (i >> 2) * 64, x_vo);
    }
    {
      asm volatile("s_waitcnt lgkmcnt(0)" : "+s"(epoch)::"memory");
      u64 z = epoch + 0x9E3779B97F4A7C15ull;
      z = (z ^ (z >> 30)) * 0xBF58476D1CE4E5B9ull;
      z = (z ^ (z >> 27)) * 0x94D049BB133111EBull;
      T = z ^ (z >> 31);
    }
    if (b < 16) {
      BAR();
      if (w == 0) mod_publish((const float*)(smem + L1_RED), lane, b >= 8, ws + WS_MODA + b * 512, T);
    }
    STAMP(0, 1)
#pragma unroll
    for (int i = 6; i < 16; i++) xr[i] = ld16(xb + (i & 3) * 8 * D_ + (i >> 2) * 64, x_vo);
    const int pid = cg * 16 + rb, tu0 = min((pid >> 5) * 11 + w, FK_ - 1), tu1 = min((pid >> 5) * 11 + 8 + w, FK_ - 1);
    const unsigned tag_lo = (unsigned)T << 16, tag_hi = (unsigned)(T >> 32);
    float xh[128];
    {
      char* slot = smem + L1_X + w * 6144;
      const unsigned vo = (unsigned)(prow * 1536 + pch * 8) * 2;
      float s1 = 0, s2 = 0;
      u32x4 xf[16];
#pragma unroll
      for (int q = 0; q < 4; q++) {
#pragma unroll
        for (int i = 3 * q; i < 3 * q + 3; i++) {
          const int idx = i * 8 + w, p = idx / 6, rem = idx - p * 6, ks = (rem / 3) * 16 + p, nb = rem % 3;
          glds(wqkv + (size_t)ks * 16 * 1536 + nb * 512 + cg * 32, vo, lds0 + L1_W + (ks * 3 + nb) * 1024);
        }
        WAIT_V(15 - q);
#pragma unroll
        for (int i = 0; i < 4; i++) {
          pin(xr[q * 4 + i]);
          const int row = i * 8 + (lane >> 3);
          *(u32x4*)(slot + row * 128 + (((lane & 7) ^ ((row >> 1) & 7)) << 4)) = xr[q * 4 + i];
        }
#pragma unroll
        for (int i = 0; i < 4; i++)
          xf[q * 4 + i] = *(const u32x4*)(slot + r31 * 128 + (((2 * i + hh) ^ ((r31 >> 1) & 7)) << 4));
#pragma unroll
        for (int i = 0; i < 4; i++)
#pragma unroll
          for (int j = 0; j < 4; j++) {
            s1 = dot2(xf[q * 4 + i][j], 0x3f803f80u, s1);
            s2 = dot2(xf[q * 4 + i][j], xf[q * 4 + i][j], s2);
          }
      }
      STAMP(0, 2)
      u32x4 tw0, tw1, v;
      {
        const u16* wp = (pch < 2 ? wg : wu) + (size_t)((pid & 31) * 16 + prow) * F_ + (pch & 1) * 8;
        tw0 = ld16p(wp + tu0 * 16);
        if (w < 3) tw1 = ld16p(wp + tu1 * 16);
      }
      v = ld16c(ws + WS_MODA, t * 16);
      s1 = sum32(s1); s2 = sum32(s2);
      if (lane < 32) *(f32x2*)(smem + L1_ST + (w * 32 + lane) * 8) = f32x2{s1, s2};
      BAR();
      const f32x2 o2 = *(const f32x2*)(smem + L1_ST + ((w ^ 4) * 32 + r31) * 8);
      const float mean = (s1 + o2[0]) * (1.0f / D_);
      const float var = fmaxf((s2 + o2[1]) * (1.0f / D_) - mean * mean, 0.f);
      const float rstd = 1.0f / sqrtf(var + 1e-5f), nm = -mean * rstd;
#pragma unroll
      for (int i = 0; i < 16; i++)
#pragma unroll
        for (int j = 0; j < 4; j++) {
          xh[i * 8 + 2 * j] = bf_lo(xf[i][j]) * rstd + nm;
          xh[i * 8 + 2 * j + 1] = bf_hi(xf[i][j]) * rstd + nm;
        }
#pragma unroll
      for (int i = 0; i < 128; i++) pin(xh[i]);
      STAMP(0, 3)
      WAIT_V(0);
      pin(tw0); pin(v);
      *(u32x4*)(slot + l16) = tw0;
      tw0 = __builtin_bit_cast(u32x4, tr_frag(slot + woff_fm, slot + woff_fm + 512));
      if (w < 3) {
        pin(tw1);
        *(u32x4*)(slot + 1024 + l16) = tw1;
        tw1 = __builtin_bit_cast(u32x4, tr_frag(slot + 1024 + woff_fm, slot + 1024 + woff_fm + 512));
      }
      for (unsigned s = 0; s < SPIN_CAP; s++) {
        const bool ok = ((v[0] ^ tag_lo) | (v[2] ^ tag_lo)) < 65536u && v[1] == tag_hi && v[3] == tag_hi;
        if (__builtin_amdgcn_ballot_w64(!ok) == 0) break;
        v = ld16c(ws + WS_MODA, t * 16);
        WAIT_V(0);
        pin(v);
      }
      const int k = (2 * t) & 511;
      *(f32x2*)(smem + L1_AB + (k >> 3) * 64 + (t < 256 ? 32 : 0) + (k & 7) * 4) = f32x2{bf_lo(v[0]), bf_lo(v[2])};
      st16c(ws + WS_WT + (size_t)(tu0 * 32 + (pid & 31)) * 1024, l16, tw0);
      if (w < 3) st16c(ws + WS_WT + (size_t)(tu1 * 32 + (pid & 31)) * 1024, l16, tw1);
    }
    BAR();
    STAMP(0, 4)
    f32x16 acc[3];
    u32x4 wn[8];
    u32x4 cn;
    const bool modb = b >= 16 && b < 48;
    {
      const char* ab = smem + L1_AB + (kh * 32 + hh) * 64;
      const char* wl = smem + L1_W + kh * 49152 + woff_std;
      f32x4 a0, a1, b0, b1;
      bf16x8 wf[2][3];
      u32x4 h[2];
#define P1_AB0(s) a0 = *(const f32x4*)(ab + (s) * 128); b0 = *(const f32x4*)(ab + (s) * 128 + 32);
#define P1_AB1(s) a1 = *(const f32x4*)(ab + (s) * 128 + 16); b1 = *(const f32x4*)(ab + (s) * 128 + 48);
#define P1_W(s, n) wf[(s) & 1][n] = tr_frag(wl + (s) * 3072 + (n) * 1024, wl + (s) * 3072 + (n) * 1024 + 256);
#define P1_H0(s)                                                                                     \
  h[(s) & 1][0] = pk2(xh[(s) * 8 + 0] * a0[0] + b0[0], xh[(s) * 8 + 1] * a0[1] + b0[1]);             \
  h[(s) & 1][1] = pk2(xh[(s) * 8 + 2] * a0[2] + b0[2], xh[(s) * 8 + 3] * a0[3] + b0[3]);
#define P1_H1(s)                                                                                     \
  h[(s) & 1][2] = pk2(xh[(s) * 8 + 4] * a1[0] + b1[0], xh[(s) * 8 + 5] * a1[1] + b1[1]);             \
  h[(s) & 1][3] = pk2(xh[(s) * 8 + 6] * a1[2] + b1[2], xh[(s) * 8 + 7] * a1[3] + b1[3]);
      P1_AB0(0) P1_AB1(0) P1_W(0, 0) P1_W(0, 1) P1_W(0, 2)
      P1_H0(0)
      SB();
      P1_AB0(1)
      P1_H1(0)
      SB();
      P1_AB1(1)
#pragma unroll
      for (int s = 0; s < 16; s++) {
        const bf16x8 a = asfrag(h[s & 1]);
        SB();
        acc[0] = mfma(wf[s & 1][0], a, s == 0 ? f32x16{} : acc[0]);
        if (s + 1 < 16) { P1_W(s + 1, 0) P1_W(s + 1, 1) P1_W(s + 1, 2) }
        SB();
        if (s + 1 < 16) { P1_H0(s + 1) }
        SB();
        acc[1] = mfma(wf[s & 1][1], a, s == 0 ? f32x16{} : acc[1]);
        if (s + 2 < 16) { P1_AB0(s + 2) }
        SB();
        if (s + 1 < 16) { P1_H1(s + 1) }
        SB();
        acc[2] = mfma(a, wf[s & 1][2], s == 0 ? f32x16{} : acc[2]);
        if (s + 2 < 16) { P1_AB1(s + 2) }
        if (s == 5 && modb) {
#pragma unroll
          for (int i = 0; i < 8; i++) wn[i] = ld16(wm_p + i * 3072, wm_vo);
          cn = ld16(cvec + w * 64, (lane >> 3) * 16);
        }
      }
      SB();
#undef P1_AB0
#undef P1_AB1
#undef P1_W
#undef P1_H0
#undef P1_H1
    }
    STAMP(0, 5)
    float fin[3][8];
    {
      char* ex = smem + L1_X + w * 6144 + l16;
      if (kh == 0) {
#pragma unroll
        for (int n = 0; n < 3; n++) {
          *(f32x4*)(ex + n * 2048) = f32x4{acc[n][8], acc[n][9], acc[n][10], acc[n][11]};
          *(f32x4*)(ex + n * 2048 + 1024) = f32x4{acc[n][12], acc[n][13], acc[n][14], acc[n][15]};
#pragma unroll
          for (int j = 0; j < 8; j++) fin[n][j] = acc[n][j];
        }
      } else {
#pragma unroll
        for (int n = 0; n < 3; n++) {
          *(f32x4*)(ex + n * 2048) = f32x4{acc[n][0], acc[n][1], acc[n][2], acc[n][3]};
          *(f32x4*)(ex + n * 2048 + 1024) = f32x4{acc[n][4], acc[n][5], acc[n][6], acc[n][7]};
#pragma unroll
          for (int j = 0; j < 8; j++) fin[n][j] = acc[n][8 + j];
        }
      }
      BAR();
      const char* ey = smem + L1_X + (w ^ 4) * 6144 + l16;
#pragma unroll
      for (int n = 0; n < 3; n++) {
        const f32x4 p0 = *(const f32x4*)(ey + n * 2048), p1 = *(const f32x4*)(ey + n * 2048 + 1024);
#pragma unroll
        for (int j = 0; j < 4; j++) { fin[n][j] += p0[j]; fin[n][4 + j] += p1[j]; }
      }
    }
    {
      const int tb = rb * 4 + tb4, head = cg * 2 + kh;
      const float sc = 0.25f * 1.4426950408889634f;
      u32x4 oq, ok, ov;
      float n2 = 0;
#pragma unroll
      for (int j = 0; j < 4; j++) {
        oq[j] = pk2(fin[0][2 * j] * sc, fin[0][2 * j + 1] * sc);
        ok[j] = pk2(fin[1][2 * j], fin[1][2 * j + 1]);
        n2 += fin[1][2 * j] * fin[1][2 * j] + fin[1][2 * j + 1] * fin[1][2 * j + 1];
        ov[j] = pk2(fin[2][2 * j], fin[2][2 * j + 1]);
      }
      st16c(ws + WS_K + (size_t)(head * 64 + tb) * 1024, l16, ok);
      st16c(ws + WS_V + (size_t)(cg * 2 * 128 + tb * 2 + kh) * 512, ((lane >> 4) & 1) * 65536 + (hh * 16 + r15) * 16, ov);
      st16c(ws + WS_Q + (size_t)(head * 64 + tb) * 1024, l16, oq);
      n2 = wave_max_nonneg(sum32(n2));
      if (lane == 0) st4c(ws + WS_KMAX + (head * 64 + tb) * 4, 0, asu(n2));
    }
    WAIT_V(0);
    BAR();
    if (t == 0) st8c(flags + (0 * 256 + pid) * 128, 0, u32x2{(unsigned)(T + 1), (unsigned)((T + 1) >> 32)});
    if (modb) {
#pragma unroll
      for (int i = 0; i < 8; i++) pin(wn[i]);
      pin(cn);
      mod_partial(wn, cn, lane, (float*)(smem + L1_RED) + w * 64);
    }
    STAMP(0, 6)
  }

  const int n0 = (g * 242) >> 3, n1 = ((g + 1) * 242) >> 3;
  const bool gu_active = n0 + loc < n1;
  const int gn = min(n0 + loc, 241), tri = gn / 11, cj = gn - tri * 11;
  const bool u_valid = gu_active && cj * 8 + w < FK_;
  const int unit = min(cj * 8 + w, FK_ - 1);
  const int rb2 = g * 4 + (loc & 3), cg8 = loc >> 2;
  const int tb2 = rb2 * 2 + (w & 1), kq = w >> 1;
  bf16x8 wfr[32];

  {
    LANE_VARS
    const int head = g * 4 + (loc & 3), qg = loc >> 2, tb = qg * 8 + w;
    if (t < 256) {
      const unsigned ov = (t & 31) == 0 ? 0x3f803f80u : 0u;
      *(u32x4*)(smem + L2_ONES + (t >> 6) * 2048 + (t & 63) * 16) = u32x4{ov, ov, ov, ov};
    }
    if (t == 0) *(unsigned*)(smem + L2_FAIL) = 0;
    if (w == 0) poll_flags(flags + ((head >> 1) * 16) * 128, r15 * 128, T + 1);
    BAR();
    STAMP(1, 0)
    if (w == 1 && b >= 16 && b < 48)
      mod_publish((const float*)(smem + L1_RED), lane, (b >> 3) == 4, ws + WS_MODB + (b - 16) * 512, T);
    u32x4 qraw = ld16c(ws + WS_Q + (size_t)(head * 64 + tb) * 1024, l16);
    unsigned km = ld4c(ws + WS_KMAX + head * 256, lane * 4);
#pragma unroll
    for (int c = 0; c < 8; c++) {
      glds_c(ws + WS_K + (size_t)(head * 64 + c * 8 + w) * 1024, l16, lds0 + (c * 8 + w) * 2048);
      glds_c(ws + WS_V + (size_t)(head * 64 + c * 8 + w) * 1024, l16, lds0 + (c * 8 + w) * 2048 + 1024);
    }
    WAIT_V(14);
    pin(qraw); pin(km);
    float q2 = 0;
#pragma unroll
    for (int j = 0; j < 4; j++) q2 = dot2(qraw[j], qraw[j], q2);
    q2 = sum32(q2);
    const float mb = sqrtf(q2 * wave_max_nonneg(asf(km))) * 1.01f;
    const bf16x8 qfr = asfrag(qraw);
    f32x16 negm, o;
#pragma unroll
    for (int r = 0; r < 16; r++) { negm[r] = -mb; o[r] = 0.f; }
    const int kaddr = lane * 16;
    const bool isv = (lane & 16) == 0;
    const int vaddr0 = isv ? (hh * 16 + r15) * 16 : L2_ONES - 1024 + (r15 == 0 ? 0 : ((r15 + 4) & 8) ? 64 : 16);
    const int vstep = isv ? 8192 : 0;
    const unsigned tag_lo = (unsigned)T << 16, tag_hi = (unsigned)(T >> 32);
    const char* const wt = ws + WS_WT + (size_t)unit * 32768;
    const char* const wtf = flags + (unit / 11) * 32 * 128;
    u32x2 wfl;
    u32x4 mg0, mg1;
    BAR();
    STAMP(1, 1)
    {
      int va = vaddr0;
      f32x16 sc[2];
      const bf16x8 kf0 = lds_frag(smem + kaddr);
      bf16x8 kf = lds_frag(smem + kaddr + 2048);
      bf16x8 vf0 = lds_frag(smem + va + 1024);
      bf16x8 vf1 = vf0;
      u32x4 p1 = u32x4{0, 0, 0, 0};
      sc[0] = mfma(kf0, qfr, negm);
#define EX(i) sc[cur][i] = __builtin_amdgcn_exp2f(sc[cur][i]);
#define ATT_TILES(T0, T1)                                                                                      \
  _Pragma("unroll") for (int tt = (T0); tt < (T1); tt++) {                                                     \
    const int cur = tt & 1, nxt = cur ^ 1;                                                                     \
    u32x4 p0;                                                                                                  \
    SB();                                                                                                      \
    if (tt + 1 < 64) sc[nxt] = mfma(kf, qfr, negm);                                                            \
    if (tt + 2 < 64) kf = lds_frag(smem + kaddr + (tt + 2) * 2048);                                            \
    if (tt >= 12 && tt < 44) wfr[(tt - 12) & 31] = asfrag(ld16c(wt + ((tt - 12) & 31) * 1024, l16));           \
    SB();                                                                                                      \
    EX(0) EX(1) EX(2) SB(); p0[0] = pk2(sc[cur][0], sc[cur][1]); SB(); EX(3) EX(4) SB();                       \
    p0[1] = pk2(sc[cur][2], sc[cur][3]); SB(); EX(5)                                                           \
    SB();                                                                                                      \
    o = mfma(vf1, asfrag(p1), o);                                         \
    vf1 = lds_frag(smem + va + (tt & 3) * 2048 + 1536);                                                        \
    SB();                                                                                                      \
    EX(6) EX(7) SB(); p0[2] = pk2(sc[cur][4], sc[cur][5]); SB(); EX(8) SB();                                   \
    p0[3] = pk2(sc[cur][6], sc[cur][7]); SB(); EX(9) EX(10) EX(11)                                             \
    SB();                                                                                                      \
    o = mfma(vf0, asfrag(p0), o);                                                                              \
    if (tt + 1 < 64) {                                                                                         \
      if (((tt + 1) & 3) == 0) va += vstep;                                                                    \
      vf0 = lds_frag(smem + va + ((tt + 1) & 3) * 2048 + 1024);                                                \
    }                                                                                                          \
    SB();                                                                                                      \
    EX(12) EX(13) SB(); p1[0] = pk2(sc[cur][8], sc[cur][9]); SB(); EX(14) EX(15) SB();                         \
    p1[1] = pk2(sc[cur][10], sc[cur][11]);                                                                     \
    p1[2] = pk2(sc[cur][12], sc[cur][13]);                                                                     \
    p1[3] = pk2(sc[cur][14], sc[cur][15]);                                                                     \
  }                                                                                                            \
  SB();
      ATT_TILES(0, 6)
      WAIT_V(10);
      BAR();
      STAMP(1, 2)
      asm volatile("global_load_dwordx2 %0, %1, %2 sc1" : "=v"(wfl) : "v"(r31 * 128), "s"(wtf) : "memory");
      ATT_TILES(6, 12)
      WAIT_V(0);
      pin(wfl);
      if (__builtin_amdgcn_ballot_w64(wfl[0] != (unsigned)(T + 1) || wfl[1] != (unsigned)((T + 1) >> 32)) != 0)
        poll_flags(wtf, r31 * 128, T + 1);
      ATT_TILES(12, 22)
      BAR();
      STAMP(1, 3)
      ATT_TILES(22, 44)
      mg0 = ld16c(ws + WS_MODB, t * 32);
      mg1 = ld16c(ws + WS_MODB + 16, t * 32);
      ATT_TILES(44, 48)
      {
        STAMP(1, 4)
        WAIT_V(0);
        pin(mg0); pin(mg1);
        for (unsigned s = 0; s < SPIN_CAP; s++) {
          const bool ok = ((mg0[0] ^ tag_lo) | (mg0[2] ^ tag_lo) | (mg1[0] ^ tag_lo) | (mg1[2] ^ tag_lo)) < 65536u &&
                          mg0[1] == tag_hi && mg0[3] == tag_hi && mg1[1] == tag_hi && mg1[3] == tag_hi;
          if (__builtin_amdgcn_ballot_w64(!ok) == 0) break;
          mg0 = ld16c(ws + WS_MODB, t * 32); mg1 = ld16c(ws + WS_MODB + 16, t * 32);
          WAIT_V(0);
          pin(mg0); pin(mg1);
        }
        const u32x2 v = u32x2{(mg0[0] & 0xffffu) | (mg0[2] << 16), (mg1[0] & 0xffffu) | (mg1[2] << 16)};
        *(u32x2*)(smem + L2_MB + t * 8) = v;
        if (t >= 128 && t < 384) {
          const int k = (t * 4) & 511, which = t < 256 ? 1 : 0;
          *(u32x2*)(smem + L2_UT + ((((k >> 4) * 2 + ((k >> 2) & 1)) * 2 + which) * 8 + ((k >> 3) & 1) * 4) * 2) = v;
        }
        const bool bad = __builtin_amdgcn_ballot_w64(!(sum32(o[8]) >= 5.4e-20f)) != 0;
        if (bad && lane == 0) *(unsigned*)(smem + L2_FAIL) = 1;
        BAR();
        if (*(const volatile unsigned*)(smem + L2_FAIL) != 0) {
          if (bad) {
            float m = -INFINITY;
            for (int u = 0; u < 64; u++) {
              const f32x16 s = mfma(lds_frag(smem + kaddr + u * 2048), qfr, f32x16{});
#pragma unroll
              for (int r = 0; r < 16; r++) m = fmaxf(m, s[r]);
            }
            m = max32(m);
#pragma unroll
            for (int r = 0; r < 16; r++) { negm[r] = -m; o[r] = 0.f; }
            attn_slow(smem, kaddr, vaddr0, vstep, 48, qfr, negm, o);
            sc[0] = mfma(lds_frag(smem + kaddr + 48 * 2048), qfr, negm);
            p1 = u32x4{0, 0, 0, 0};
          }
          BAR();
        }
        const unsigned vo = (unsigned)(prow * D_ + pch * 8) * 2;
#pragma unroll
        for (int i = 0; i < 8; i++) {
          const int pi = i * 8 + w;
          glds(wo + (size_t)(pi >> 1) * 16 * D_ + cg8 * 64 + (pi & 1) * 32, vo, lds0 + pi * 1024);
        }
      }
      ATT_TILES(48, 64)
#undef ATT_TILES
#undef EX
      o = mfma(vf1, asfrag(p1), o);
    }
#pragma unroll
    for (int s = 0; s < 32; s++) pin(wfr[s]);
    STAMP(1, 5)
    const float inv = 1.0f / sum32(o[8]);
    u32x4 r;
#pragma unroll
    for (int j = 0; j < 4; j++) r[j] = pk2(o[2 * j] * inv, o[2 * j + 1] * inv);
    st16c(ws + WS_O + (size_t)(tb * 32 + head) * 1024, l16, r);
    WAIT_V(0);
    BAR();
    if (t == 0) st8c(flags + (1 * 256 + qg * 32 + head) * 128, 0, u32x2{(unsigned)(T + 2), (unsigned)((T + 2) >> 32)});
    STAMP(1, 6)
  }

  u32x4 x1p;
  u32x2 gb0, gb1;
  {
    LANE_VARS
    const int pc = cg8 * 64 + kq * 16 + hh * 4;
    u32x2 xv0 = ld8(x + (size_t)tb2 * 32 * D_, (unsigned)(r31 * D_ + pc) * 2);
    u32x2 xv1 = ld8(x + (size_t)tb2 * 32 * D_ + 8, (unsigned)(r31 * D_ + pc) * 2);
    const char* utb = smem + L2_UT + (hh * 2 + (lane & 1)) * 16;
    float* const up = (float*)(smem + L4_UT) + ((w * 2 + (lane & 1)) * 2 + hh) * 16;
    {
      f32x16 ut = {};
#pragma unroll
      for (int s = 0; s < 16; s++) ut = mfma(wfr[s], lds_frag(utb + s * 64), ut);
      if (r31 < 2) {
#pragma unroll
        for (int q = 0; q < 4; q++) *(f32x4*)(up + q * 4) = f32x4{ut[q * 4], ut[q * 4 + 1], ut[q * 4 + 2], ut[q * 4 + 3]};
      }
    }
    poll_flags(flags + (1 * 256 + g * 32 + kq * 8) * 128, (lane & 7) * 128, T + 2);
    STAMP(2, 0)
    u32x4 af[8];
#pragma unroll
    for (int i = 0; i < 8; i++) af[i] = ld16c(ws + WS_O + (size_t)(tb2 * 32 + kq * 8 + i) * 1024, l16);
    f32x16 acc[2];
#pragma unroll
    for (int r = 0; r < 16; r++) acc[0][r] = acc[1][r] = 0.f;
    const char* wl = smem + kq * 16384 + woff_fm;
    bf16x8 w0 = tr_frag(wl, wl + 512), w1 = tr_frag(wl + 1024, wl + 1536);
#pragma unroll
    for (int i = 0; i < 8; i++) {
      bf16x8 n0f = w0, n1f = w1;
      if (i + 1 < 8) {
        n0f = tr_frag(wl + (i + 1) * 2048, wl + (i + 1) * 2048 + 512);
        n1f = tr_frag(wl + (i + 1) * 2048 + 1024, wl + (i + 1) * 2048 + 1536);
      }
      WAIT_V(7 - i);
      pin(af[i]);
      acc[0] = mfma(w0, asfrag(af[i]), acc[0]);
      acc[1] = mfma(w1, asfrag(af[i]), acc[1]);
      w0 = n0f; w1 = n1f;
    }
    STAMP(2, 1)
    float fin[8];
#define KQ_REDUCE(RED)                                                                                          \
  {                                                                                                             \
    char* rw = smem + (RED) + (w & 1) * 32768 + kq * 2048 + l16;                                                \
    _Pragma("unroll") for (int p = 0; p < 4; p++) {                                                             \
      const f32x16& a = acc[p >> 1];                                                                            \
      const int e = (p & 1) * 8;                                                                                \
      if (p != kq) {                                                                                            \
        *(f32x4*)(rw + p * 8192) = f32x4{a[e], a[e + 1], a[e + 2], a[e + 3]};                                   \
        *(f32x4*)(rw + p * 8192 + 1024) = f32x4{a[e + 4], a[e + 5], a[e + 6], a[e + 7]};                        \
      } else {                                                                                                  \
        _Pragma("unroll") for (int j = 0; j < 8; j++) fin[j] = a[e + j];                                        \
      }                                                                                                         \
    }                                                                                                           \
    BAR();                                                                                                      \
    const char* rr = smem + (RED) + (w & 1) * 32768 + kq * 8192 + l16;                                          \
    _Pragma("unroll") for (int p = 1; p < 4; p++) {                                                             \
      const int src = (kq + p) & 3;                                                                             \
      const f32x4 p0 = *(const f32x4*)(rr + src * 2048), p1 = *(const f32x4*)(rr + src * 2048 + 1024);          \
      _Pragma("unroll") for (int j = 0; j < 4; j++) { fin[j] += p0[j]; fin[4 + j] += p1[j]; }                   \
    }                                                                                                           \
  }
    KQ_REDUCE(L3_RED)
    pin(xv0); pin(xv1);
    const char* mbp = smem + L2_MB + pc * 2;
    const u32x2 ga0 = *(const u32x2*)mbp, ga1 = *(const u32x2*)(mbp + 16);
    const u32x2 a20 = *(const u32x2*)(mbp + 2048), a21 = *(const u32x2*)(mbp + 2064);
    gb0 = *(const u32x2*)(mbp + 3072); gb1 = *(const u32x2*)(mbp + 3088);
    const u32x4 xv = u32x4{xv0[0], xv0[1], xv1[0], xv1[1]}, ga = u32x4{ga0[0], ga0[1], ga1[0], ga1[1]};
    const u32x4 a2 = u32x4{a20[0], a20[1], a21[0], a21[1]};
    float x1[8], sum = 0;
    u32x4 y;
#pragma unroll
    for (int j = 0; j < 4; j++) {
      const unsigned p = pk2(bf_lo(xv[j]) + bf_lo(ga[j]) * fin[2 * j], bf_hi(xv[j]) + bf_hi(ga[j]) * fin[2 * j + 1]);
      x1p[j] = p;
      x1[2 * j] = bf_lo(p); x1[2 * j + 1] = bf_hi(p);
      sum += x1[2 * j] + x1[2 * j + 1];
      y[j] = pk2(x1[2 * j] * bf_lo(a2[j]), x1[2 * j + 1] * bf_hi(a2[j]));
    }
    st16c(ws + WS_Y + (size_t)(tb2 * 32 + cg8 * 4 + kq) * 1024, l16, y);
    const float mean = sum32(sum) * (1.0f / 16.0f);
    float m2 = 0;
#pragma unroll
    for (int j = 0; j < 8; j++) { const float d = x1[j] - mean; m2 += d * d; }
    m2 = sum32(m2);
    if (lane < 32) st8c(ws + WS_ST2 + (size_t)(tb2 * 32 + cg8 * 4 + kq) * 256, lane * 8, u32x2{asu(mean), asu(m2)});
    WAIT_V(0);
    BAR();
    if (t == 0) st8c(flags + (2 * 256 + rb2 * 8 + cg8) * 128, 0, u32x2{(unsigned)(T + 3), (unsigned)((T + 3) >> 32)});
    STAMP(2, 2)
    {
      f32x16 ut = {};
#pragma unroll
      for (int s = 16; s < 32; s++) ut = mfma(wfr[s], lds_frag(utb + s * 64), ut);
      if (r31 < 2) {
#pragma unroll
        for (int q = 0; q < 4; q++) {
          const float c = q < 2 ? -1.4426950408889634f : 1.0f;
          const f32x4 v = *(const f32x4*)(up + q * 4);
          *(f32x4*)(up + q * 4) = f32x4{(ut[q * 4] + v[0]) * c, (ut[q * 4 + 1] + v[1]) * c, (ut[q * 4 + 2] + v[2]) * c,
                                        (ut[q * 4 + 3] + v[3]) * c};
        }
      }
    }
  }

#define WD_PIECE(pi)                                                                                    \
  {                                                                                                     \
    const int pi_ = min((pi), 155), sl = pi_ >> 1, q_ = sl / 19;                                        \
    const int ks_ = sl < 76 ? q_ * 21 + (sl - q_ * 19) + 2 : sl + 8;                                    \
    glds(wd + (size_t)ks_ * 16 * D_ + cg8 * 64 + (pi_ & 1) * 32, wd_vo, lds0 + pi_ * 1024);             \
  }
  u32x4 wdr[4];
  {
    LANE_VARS
    const unsigned wd_vo = (unsigned)(prow * D_ + pch * 8) * 2;
    if (w == 0) {
      const int rbA = (3 * tri) >> 1, rbB = min((3 * tri + 2) >> 1, 31);
      poll_flags(flags + 2 * 256 * 128, (((lane & 8) ? rbB : rbA) * 8 + (lane & 7)) * 128, T + 3);
    }
    BAR();
    STAMP(3, 0)
#define Y_PIECE(j, i)                                                                                   \
  glds_c(ws + WS_Y + (size_t)min(3 * tri + (j), 63) * 32768 + w * 1024 + (i) * 8192, l16,               \
         lds0 + (j) * 32768 + ((i) * 8 + w) * 1024);
#pragma unroll
    for (int i = 0; i < 8; i++) Y_PIECE(i >> 2, i & 3)
    u32x4 sr[8];
    if (w < 3) {
      const char* sp = ws + WS_ST2 + (size_t)min(3 * tri + w, 63) * 8192;
#pragma unroll
      for (int i = 0; i < 8; i++) sr[i] = ld16c(sp + i * 1024, l16);
    }
    f32x16 acc[2];
#define GU_STEP(j)                                                                                      \
  {                                                                                                     \
    const char* yl = smem + (j) * 32768 + l16;                                                          \
    const float* up = (const float*)(smem + L4_UT) + (w * 4 + hh) * 16;                                 \
    const f32x16& a = acc[((j) + 1) & 1];                                                               \
    bf16x8 yf[4];                                                                                       \
    f32x4 ug, uu, tg, tu;                                                                               \
    float rs = 0, nm = 0, rg = 0, hv[2];                                                                \
    u32x4 hp;                                                                                           \
    if ((j) < 3) {                                                                                      \
      _Pragma("unroll") for (int i = 0; i < 4; i++) yf[i] = lds_frag(yl + i * 1024);                    \
    }                                                                                                   \
    if ((j) > 0) {                                                                                      \
      const f32x2 st = *(const f32x2*)(smem + L4_ST + ((j) - 1) * 256 + r31 * 8);                       \
      rs = st[0]; nm = st[1]; rg = -1.4426950408889634f * rs;                                           \
    }                                                                                                   \
    _Pragma("unroll") for (int s = 0; s < 32; s++) {                                                    \
      if ((j) < 3) {                                                                                    \
        SB();                                                                                           \
        if (s == 0) acc[(j) & 1] = mfma(wfr[s], yf[s & 3], f32x16{});                                   \
        else acc[(j) & 1] = mfma(wfr[s], yf[s & 3], acc[(j) & 1]);                                      \
        if (s + 4 < 32) yf[s & 3] = lds_frag(yl + (s + 4) * 1024);                                      \
        if ((j) == 0 && s < 8 && (s & 1)) Y_PIECE(2, s >> 1)                                            \
        if ((j) == 0 && s >= 8 && s % 3 == 0) WD_PIECE(96 + (s / 3 - 3) * 8 + w)                        \
        if ((j) > 0 && (s & 7) == 1) WD_PIECE(((j) - 1) * 32 + (s >> 3) * 8 + w)                        \
      }                                                                                                 \
      if ((j) > 0) {                                                                                    \
        const int q = s >> 4, i = (s >> 2) & 3;                                                         \
        if ((s & 15) == 0) {                                                                            \
          ug = *(const f32x4*)(up + q * 4); uu = *(const f32x4*)(up + 8 + q * 4);                       \
          tg = *(const f32x4*)(up + 32 + q * 4); tu = *(const f32x4*)(up + 40 + q * 4);                 \
        }                                                                                               \
        if ((s & 3) == 3) {                                                                             \
          const float gt = a[q * 4 + i] * rg + (nm * ug[i] + tg[i]);                                    \
          const float uv = a[8 + q * 4 + i] * rs + (nm * uu[i] + tu[i]);                                \
          hv[i & 1] = uv * __builtin_amdgcn_rcpf(1.0f + __builtin_amdgcn_exp2f(gt));                    \
          if (i & 1) hp[q * 2 + (i >> 1)] = pk2(hv[0], hv[1]);                                          \
        }                                                                                               \
      }                                                                                                 \
    }                                                                                                   \
    if ((j) > 0 && u_valid && 3 * tri + (j) - 1 < 64)                                                   \
      st16c(ws + WS_HH + (size_t)((3 * tri + (j) - 1) * FK_ + unit) * 1024, l16, hp);                   \
    SB();                                                                                               \
  }
    if (w < 3) WAIT_V(12);
    else WAIT_V(4);
    BAR();
    STAMP(3, 1)
    GU_STEP(0)
    WAIT_V(12);
    if (w < 3) {
      float ma = 0, mb = 0;
#pragma unroll
      for (int i = 0; i < 8; i++) { pin(sr[i]); ma += asf(sr[i][0]); mb += asf(sr[i][2]); }
      ma = sum32(sum16(ma)) * (1.0f / 32.0f);
      mb = sum32(sum16(mb)) * (1.0f / 32.0f);
      float qa = 0, qb = 0;
#pragma unroll
      for (int i = 0; i < 8; i++) {
        const float da = asf(sr[i][0]) - ma, db = asf(sr[i][2]) - mb;
        qa += asf(sr[i][1]) + 16.0f * da * da;
        qb += asf(sr[i][3]) + 16.0f * db * db;
      }
      qa = sum32(sum16(qa)); qb = sum32(sum16(qb));
      const float ra = 1.0f / sqrtf(qa * (1.0f / D_) + 1e-5f), rb = 1.0f / sqrtf(qb * (1.0f / D_) + 1e-5f);
      if (lane < 16) *(f32x4*)(smem + L4_ST + w * 256 + lane * 16) = f32x4{ra, -ma * ra, rb, -mb * rb};
    }
    BAR();
    STAMP(3, 2)
    GU_STEP(1)
    if (u_valid) WAIT_V(13);
    else WAIT_V(12);
    BAR();
    STAMP(3, 3)
    GU_STEP(2)
    STAMP(3, 4)
    GU_STEP(3)
#undef GU_STEP
#undef Y_PIECE
#pragma unroll
    for (int i = 0; i < 4; i++)
      wdr[i] = ld16(wd + (size_t)(kq * 21 + (i >> 1)) * 16 * D_ + cg8 * 64 + (i & 1) * 32, wd_vo);
    WAIT_V(4);
    BAR();
    if (t == 0 && gu_active) st8c(flags + (3 * 256 + gn) * 128, 0, u32x2{(unsigned)(T + 4), (unsigned)((T + 4) >> 32)});
    STAMP(3, 5)
  }

  {
    LANE_VARS
    const unsigned wd_vo = (unsigned)(prow * D_ + pch * 8) * 2;
#pragma unroll
    for (int i = 0; i < 4; i++) WD_PIECE(64 + i * 8 + w)
    bf16x8 wdf[4];
    {
      char* sl = smem + L4_UT + w * 512;
      WAIT_V(4);
#pragma unroll
      for (int i = 0; i < 4; i++) {
        pin(wdr[i]);
        if (hh == 0) *(u32x4*)(sl + r31 * 16) = wdr[i];
        const v4i16_t a = __builtin_amdgcn_ds_read_tr16_b64_v4i16((LDS_AS v4i16_t*)(sl + woff_fm));
        asm volatile("" ::: "memory");
        if (hh == 1) *(u32x4*)(sl + r31 * 16) = wdr[i];
        const v4i16_t c = __builtin_amdgcn_ds_read_tr16_b64_v4i16((LDS_AS v4i16_t*)(sl + woff_fm));
        asm volatile("" ::: "memory");
        wdf[i] = __builtin_bit_cast(bf16x8, __builtin_shufflevector(a, c, 0, 1, 2, 3, 4, 5, 6, 7));
      }
    }
    WAIT_V(0);
    BAR();
    {
      const int c0 = (kq * 21) >> 3, c1 = (kq * 21 + 20) >> 3;
      const int cjl = (lane & 4) && kq < 2 ? 10 : min(c0 + (lane & 3), c1);
      poll_flags(flags + (3 * 256 + (tb2 / 3) * 11) * 128, cjl * 128, T + 4);
    }
    STAMP(4, 0)
    const char* hp = ws + WS_HH + (size_t)(tb2 * FK_ + kq * 21) * 1024;
    u32x4 ax = u32x4{0, 0, 0, 0};
    if (kq < 2) ax = ld16c(ws + WS_HH + (size_t)(tb2 * FK_ + 84 + kq) * 1024, l16);
    u32x4 af[21];
#pragma unroll
    for (int i = 0; i < 21; i++) af[i] = ld16c(hp + i * 1024, l16);
    f32x16 acc[2];
#pragma unroll
    for (int r = 0; r < 16; r++) acc[0][r] = acc[1][r] = 0.f;
    const char* wl = smem + kq * 19 * 2048 + woff_fm;
    bf16x8 w0 = wdf[0], w1 = wdf[1];
#pragma unroll
    for (int i = 0; i < 21; i++) {
      bf16x8 n0f = w0, n1f = w1;
      if (i == 0) { n0f = wdf[2]; n1f = wdf[3]; }
      else if (i + 1 < 21) {
        n0f = tr_frag(wl + (i - 1) * 2048, wl + (i - 1) * 2048 + 512);
        n1f = tr_frag(wl + (i - 1) * 2048 + 1024, wl + (i - 1) * 2048 + 1536);
      }
      WAIT_V(20 - i);
      pin(af[i]);
      acc[0] = mfma(w0, asfrag(af[i]), acc[0]);
      acc[1] = mfma(w1, asfrag(af[i]), acc[1]);
      w0 = n0f; w1 = n1f;
    }
    if (kq < 2) {
      pin(ax);
      const char* wx = smem + (76 + kq) * 2048 + woff_fm;
      acc[0] = mfma(tr_frag(wx, wx + 512), asfrag(ax), acc[0]);
      acc[1] = mfma(tr_frag(wx + 1024, wx + 1536), asfrag(ax), acc[1]);
    }
    STAMP(4, 1)
    BAR();
    float fin[8];
    KQ_REDUCE(0)
#undef KQ_REDUCE
    const u32x4 gb = u32x4{gb0[0], gb0[1], gb1[0], gb1[1]};
    unsigned pk[4];
#pragma unroll
    for (int j = 0; j < 4; j++)
      pk[j] = pk2(bf_lo(x1p[j]) + bf_lo(gb[j]) * fin[2 * j], bf_hi(x1p[j]) + bf_hi(gb[j]) * fin[2 * j + 1]);
    auto r0 = __builtin_amdgcn_permlane32_swap(pk[0], pk[2], false, false);
    auto r1 = __builtin_amdgcn_permlane32_swap(pk[1], pk[3], false, false);
    *(u32x4*)(out + (size_t)(tb2 * 32 + r31) * D_ + cg8 * 64 + kq * 16 + hh * 8) = u32x4{r0[0], r1[0], r0[1], r1[1]};
    if (b == 0 && t == 0) *(u64*)(ws + WS_EPOCH) = epoch + 1;
    STAMP(4, 2)
  }
}

extern "C" void kernel_launch(void* const* d_in, const int* in_sizes, int n_in, void* d_out, int out_size, void* d_ws,
                              size_t ws_size, hipStream_t stream) {
  fused_block<<<256, 512, 0, stream>>>((const u16*)d_in[0], (const u16*)d_in[1], (const u16*)d_in[2],
                                       (const u16*)d_in[4], (const u16*)d_in[5], (const u16*)d_in[6],
                                       (const u16*)d_in[7], (const u16*)d_in[8], (u16*)d_out, (char*)d_ws);
}
```
